```python
import torch

def _softmax(x, axis=-1):
    return torch.softmax(x, dim=axis)
def _gelu(x):
    return torch.nn.functional.gelu(x, approximate="tanh")
def _silu(x):
    return torch.nn.functional.silu(x)
def _sigmoid(x):
    return torch.sigmoid(x)
def _layernorm(x, axis=-1, eps=1e-5):
    m = x.mean(dim=axis, keepdim=True)
    v = x.var(dim=axis, keepdim=True, unbiased=False)
    return (x - m) / torch.sqrt(v + eps)
def _scatter_add(tgt, idx, src, axis=0):
    return tgt.clone().index_add_(axis, idx, src)
def _segment_sum(src, seg, n):
    out = torch.zeros((n,) + src.shape[1:], dtype=src.dtype)
    return out.index_add_(0, seg, src)
def _pad(x, pw):
    flat = tuple(v for pair in reversed(pw) for v in pair)
    return torch.nn.functional.pad(x, flat)

kernel_name = 'sequential_preLN__mha_block_cached__swiglu_ffn__decode'

def setup_inputs():

    _w = (lambda a: (lambda t: t.to(torch.bfloat16) if t.is_floating_point() else t)(torch.as_tensor(a)))
    def _A_setup():
        B, Sq, D = 2, 1, 512
        H, Sk, Dh = 32, 16384, 128
        return {
            "x": _w((lambda *s: torch.randn(*s, dtype=torch.bfloat16) * 0.05)(B, Sq, D)),
            "k_cache": _w((lambda *s: torch.randn(*s, dtype=torch.bfloat16) * 3.2)(B, H, Sk, Dh)),
            "v_cache": _w((lambda *s: torch.randn(*s, dtype=torch.bfloat16) * 0.4)(B, H, Sk, Dh)),
            "wq": _w((lambda *s: torch.randn(*s, dtype=torch.bfloat16) * 3.2)(D, H * Dh) * (D ** -0.5)),
            "wo": _w((lambda *s: torch.randn(*s, dtype=torch.bfloat16) * 0.4)(H * Dh, D) * ((H * Dh) ** -0.5)),
        }
    _A_in = _A_setup()

    def _B_setup():
        B, S, D, F = 2, 1, 512, 1792
        return {
            "x": _w((lambda *s: torch.randn(*s, dtype=torch.bfloat16) * 0.05)(B, S, D)),
            "w_gate": _w((lambda *s: torch.randn(*s, dtype=torch.bfloat16) * 0.05)(D, F)),
            "w_up": _w((lambda *s: torch.randn(*s, dtype=torch.bfloat16) * 0.05)(D, F)),
            "w_down": _w((lambda *s: torch.randn(*s, dtype=torch.bfloat16) * 0.05)(F, D)),
        }
    _B_in = _B_setup()

    return {
        "A_x": _A_in["x"],
        "A_k_cache": _A_in["k_cache"],
        "A_v_cache": _A_in["v_cache"],
        "A_wq": _A_in["wq"],
        "A_wo": _A_in["wo"],
        "B_x": _B_in["x"],
        "B_w_gate": _B_in["w_gate"],
        "B_w_up": _B_in["w_up"],
        "B_w_down": _B_in["w_down"],
    }

def reference(A_x, A_k_cache, A_v_cache, A_wq, A_wo, B_x, B_w_gate, B_w_up, B_w_down):
    def _A_ref(x, k_cache, v_cache, wq, wo):
        B, Sq, D = x.shape
        H, Dh = 32, 128
        q = (lambda x, *d: x.permute(*d))((x @ wq).reshape(B, Sq, H, Dh), 0, 2, 1, 3)
        scores = torch.einsum("bhqd,bhkd->bhqk", q, k_cache) * (Dh ** -0.5)
        p = _softmax(scores, axis=-1)
        o = torch.einsum("bhqk,bhkd->bhqd", p, v_cache)
        return (lambda x, *d: x.permute(*d))(o, 0, 2, 1, 3).reshape(B, Sq, H * Dh) @ wo

    def _B_ref(x, w_gate, w_up, w_down):
        g = _silu(x @ w_gate)
        u = x @ w_up
        return (g * u) @ w_down

    x = A_x
    h = x + _A_ref(_layernorm(x, axis=-1), A_k_cache, A_v_cache, A_wq, A_wo)
    h2 = h + _B_ref(_layernorm(h, axis=-1), B_w_gate, B_w_up, B_w_down)
    return h2
```

```cpp
#include <hip/hip_runtime.h>
#include <hip/hip_bf16.h>
#include <stdint.h>

typedef unsigned u32x4 __attribute__((ext_vector_type(4)));
typedef float f32x4 __attribute__((ext_vector_type(4)));
typedef __bf16 bf16x2 __attribute__((ext_vector_type(2)));

constexpr int NB = 2, DM = 512, NH = 32, SK = 16384, DH = 128, FF = 1792;
constexpr int NSPLIT = 4;
constexpr int KPS = SK / NSPLIT;
constexpr int NWAVE = 8, NTHR = 512;
constexpr int KPW = KPS / NWAVE;
constexpr int PAGE = 16;
constexpr int NPAGE = KPW / PAGE;

__device__ __forceinline__ float bf2f(unsigned short b) { return __builtin_bit_cast(float, (unsigned)b << 16); }
__device__ __forceinline__ float rbf(float x) { return __bfloat162float(__float2bfloat16(x)); }
__device__ __forceinline__ unsigned short f2bf(float x) { return __builtin_bit_cast(unsigned short, __float2bfloat16(x)); }
__device__ __forceinline__ float lo16(unsigned u) { return __builtin_bit_cast(float, u << 16); }
__device__ __forceinline__ float hi16(unsigned u) { return __builtin_bit_cast(float, u & 0xffff0000u); }
__device__ __forceinline__ float dot2(unsigned k, unsigned q, float acc) {
  return __builtin_amdgcn_fdot2_f32_bf16(__builtin_bit_cast(bf16x2, k), __builtin_bit_cast(bf16x2, q), acc, false);
}
template <int CTRL> __device__ __forceinline__ float dpp(float x) {
  return __builtin_bit_cast(float, __builtin_amdgcn_mov_dpp(__builtin_bit_cast(int, x), CTRL, 0xf, 0xf, true));
}
constexpr int XOR1 = 0xB1, XOR2 = 0x4E, XOR7 = 0x141, XOR8 = 0x128;
__device__ __forceinline__ float row16_sum(float x) {
  x += dpp<XOR1>(x); x += dpp<XOR2>(x); x += dpp<XOR7>(x); x += dpp<XOR8>(x); return x;
}
__device__ __forceinline__ float wave_sum(float x) {
  x = row16_sum(x);
  x += __shfl_xor(x, 16); x += __shfl_xor(x, 32);
  return x;
}

__device__ __forceinline__ void ln512_wave(const float* in, float* out, int lane) {
  float v[8]; float s = 0.f;
#pragma unroll
  for (int i = 0; i < 8; ++i) { v[i] = in[lane + 64 * i]; s += v[i]; }
  s = wave_sum(s);
  const float mean = s * (1.0f / 512.0f);
  float ss = 0.f;
#pragma unroll
  for (int i = 0; i < 8; ++i) { const float d = v[i] - mean; ss += d * d; }
  ss = wave_sum(ss);
  const float var = rbf(ss * (1.0f / 512.0f));
  const float mb = rbf(mean);
  const float den = rbf(sqrtf(rbf(var + 1e-5f)));
#pragma unroll
  for (int i = 0; i < 8; ++i) out[lane + 64 * i] = rbf(rbf(v[i] - mb) / den);
}

constexpr size_t WS_PML = 0;
constexpr size_t WS_PO = 1024;
constexpr size_t WS_Y = WS_PO + (size_t)NB * NH * NSPLIT * DH;
constexpr size_t WS_H = WS_Y + (size_t)NH * NB * DM;
constexpr size_t WS_ACT = WS_H + NB * DM;

__global__ void __launch_bounds__(NTHR, 2) k_attn(const unsigned short* __restrict__ x, const unsigned short* __restrict__ kc,
                                                  const unsigned short* __restrict__ vc, const unsigned short* __restrict__ wq,
                                                  float* __restrict__ ws) {
  __shared__ __attribute__((aligned(16))) float lds[32 * 132 + 1024 + 64];
  float* red = lds;
  float* xs = lds + 32 * 132;
  float* lnx = xs + 512;
  unsigned* qs = (unsigned*)(lnx + 512);
  const int tid = threadIdx.x, lane = tid & 63, wave = __builtin_amdgcn_readfirstlane(tid >> 6), r = lane >> 4, c = lane & 15;
  const int bid = blockIdx.x, xcd = bid & 7, j = bid >> 3;
  const int h = xcd * 4 + (j >> 3), b = (j >> 2) & 1, sp = j & 3;
  const size_t base = ((size_t)(b * NH + h) * SK + (size_t)sp * KPS + (size_t)wave * KPW) * DH;
  const u32x4* kp = (const u32x4*)(kc + base) + r * 16 + c;
  const u32x4* vp = (const u32x4*)(vc + base) + r * 16 + c;
  constexpr int DEPTH = 4;
  u32x4 Kb[DEPTH][4], Vb[DEPTH][4];
#pragma unroll
  for (int d = 0; d < DEPTH - 1; ++d)
#pragma unroll
    for (int t = 0; t < 4; ++t) {
      Kb[d][t] = __builtin_nontemporal_load(kp + (d * 4 + t) * 64);
      Vb[d][t] = __builtin_nontemporal_load(vp + (d * 4 + t) * 64);
    }
  xs[tid] = bf2f(x[b * DM + tid]);
  __syncthreads();
  if (wave == 0) ln512_wave(xs, lnx, lane);
  __syncthreads();
  {
    const int cg = tid & 15, rg = tid >> 4;
    float a[8];
#pragma unroll
    for (int k = 0; k < 8; ++k) a[k] = 0.f;
    const u32x4* wp = (const u32x4*)(wq + (size_t)h * DH) + cg;
#pragma unroll
    for (int half = 0; half < 2; ++half) {
      u32x4 w[8];
#pragma unroll
      for (int i = 0; i < 8; ++i) w[i] = wp[(size_t)(rg + 32 * (half * 8 + i)) * (NH * DH / 8)];
#pragma unroll
      for (int i = 0; i < 8; ++i) {
        const float xv = lnx[rg + 32 * (half * 8 + i)];
#pragma unroll
        for (int k = 0; k < 4; ++k) { a[2 * k] = fmaf(xv, lo16(w[i][k]), a[2 * k]); a[2 * k + 1] = fmaf(xv, hi16(w[i][k]), a[2 * k + 1]); }
      }
    }
#pragma unroll
    for (int k = 0; k < 8; ++k) red[rg * 132 + cg * 8 + k] = a[k];
  }
  __syncthreads();
  if (tid < 64) {
    float s0 = 0.f, s1 = 0.f;
#pragma unroll
    for (int g = 0; g < 32; ++g) { s0 += red[g * 132 + 2 * tid]; s1 += red[g * 132 + 2 * tid + 1]; }
    qs[tid] = (unsigned)f2bf(s0) | ((unsigned)f2bf(s1) << 16);
  }
  __syncthreads();
  const u32x4 q = *(const u32x4*)(qs + 4 * c);
  constexpr float SCALE = 0.08838834764831845f, L2E = 1.4426950408889634f;
  float m = -INFINITY, l = 0.f, acc[8];
#pragma unroll
  for (int k = 0; k < 8; ++k) acc[k] = 0.f;
  for (int p0 = 0; p0 < NPAGE; p0 += DEPTH) {
#pragma unroll
    for (int d = 0; d < DEPTH; ++d) {
      const int p = p0 + d, pn = p + DEPTH - 1;
      constexpr int dn_ = 0; (void)dn_;
      if (pn < NPAGE) {
#pragma unroll
        for (int t = 0; t < 4; ++t) {
          Kb[(d + DEPTH - 1) % DEPTH][t] = __builtin_nontemporal_load(kp + (pn * 4 + t) * 64);
          Vb[(d + DEPTH - 1) % DEPTH][t] = __builtin_nontemporal_load(vp + (pn * 4 + t) * 64);
        }
      }
      float s[4];
#pragma unroll
      for (int t = 0; t < 4; ++t) {
        float dd = 0.f;
#pragma unroll
        for (int k = 0; k < 4; ++k) dd = dot2(Kb[d][t][k], q[k], dd);
        dd = row16_sum(dd);
        s[t] = rbf(rbf(dd) * SCALE) * L2E;
      }
      const float pm = fmaxf(fmaxf(s[0], s[1]), fmaxf(s[2], s[3]));
      const float mn = fmaxf(m, pm);
      const float alpha = __builtin_amdgcn_exp2f(m - mn);
      m = mn;
      float pp[4];
#pragma unroll
      for (int t = 0; t < 4; ++t) pp[t] = __builtin_amdgcn_exp2f(s[t] - mn);
      l = fmaf(l, alpha, (pp[0] + pp[1]) + (pp[2] + pp[3]));
#pragma unroll
      for (int k = 0; k < 8; ++k) acc[k] *= alpha;
#pragma unroll
      for (int t = 0; t < 4; ++t)
#pragma unroll
        for (int k = 0; k < 4; ++k) {
          acc[2 * k] = fmaf(pp[t], lo16(Vb[d][t][k]), acc[2 * k]);
          acc[2 * k + 1] = fmaf(pp[t], hi16(Vb[d][t][k]), acc[2 * k + 1]);
        }
    }
  }
  __syncthreads();
  {
    float* rec = red + (wave * 4 + r) * 132;
#pragma unroll
    for (int k = 0; k < 8; ++k) rec[4 + 8 * c + k] = acc[k];
    if (c == 0) { rec[0] = m; rec[1] = l; }
  }
  __syncthreads();
  if (tid < DH) {
    float M = -INFINITY;
#pragma unroll
    for (int g = 0; g < 32; ++g) M = fmaxf(M, red[g * 132]);
    float L = 0.f, o = 0.f;
#pragma unroll
    for (int g = 0; g < 32; ++g) {
      const float w = __builtin_amdgcn_exp2f(red[g * 132] - M);
      L = fmaf(w, red[g * 132 + 1], L);
      o = fmaf(w, red[g * 132 + 4 + tid], o);
    }
    const size_t slot = (size_t)(b * NH + h) * NSPLIT + sp;
    ws[WS_PO + slot * DH + tid] = o;
    if (tid == 0) { ws[WS_PML + slot * 2] = M; ws[WS_PML + slot * 2 + 1] = L; }
  }
}

__global__ void __launch_bounds__(256) k_wo(const unsigned short* __restrict__ wo, float* __restrict__ ws) {
  __shared__ __attribute__((aligned(16))) float os[NB * DH];
  __shared__ __attribute__((aligned(16))) float red[16 * NB * 128];
  const int tid = threadIdx.x, h = blockIdx.x >> 2, cq = blockIdx.x & 3;
  {
    const int b = tid >> 7, d = tid & 127;
    const size_t slot = (size_t)(b * NH + h) * NSPLIT;
    float M = -INFINITY;
#pragma unroll
    for (int s = 0; s < NSPLIT; ++s) M = fmaxf(M, ws[WS_PML + (slot + s) * 2]);
    float L = 0.f, o = 0.f;
#pragma unroll
    for (int s = 0; s < NSPLIT; ++s) {
      const float w = __builtin_amdgcn_exp2f(ws[WS_PML + (slot + s) * 2] - M);
      L = fmaf(w, ws[WS_PML + (slot + s) * 2 + 1], L);
      o = fmaf(w, ws[WS_PO + (slot + s) * DH + d], o);
    }
    os[tid] = rbf(o / L);
  }
  __syncthreads();
  const int cg = tid & 15, rg = tid >> 4;
  float a[NB][8];
#pragma unroll
  for (int k = 0; k < 8; ++k) { a[0][k] = 0.f; a[1][k] = 0.f; }
  const u32x4* wp = (const u32x4*)(wo + (size_t)h * DH * DM + cq * 128) + cg;
  u32x4 w[8];
#pragma unroll
  for (int i = 0; i < 8; ++i) w[i] = wp[(size_t)(rg + 16 * i) * (DM / 8)];
#pragma unroll
  for (int i = 0; i < 8; ++i) {
    const float o0 = os[rg + 16 * i], o1 = os[DH + rg + 16 * i];
#pragma unroll
    for (int k = 0; k < 4; ++k) {
      a[0][2 * k] = fmaf(o0, lo16(w[i][k]), a[0][2 * k]); a[0][2 * k + 1] = fmaf(o0, hi16(w[i][k]), a[0][2 * k + 1]);
      a[1][2 * k] = fmaf(o1, lo16(w[i][k]), a[1][2 * k]); a[1][2 * k + 1] = fmaf(o1, hi16(w[i][k]), a[1][2 * k + 1]);
    }
  }
#pragma unroll
  for (int k = 0; k < 8; ++k) { red[(rg * NB + 0) * 128 + cg * 8 + k] = a[0][k]; red[(rg * NB + 1) * 128 + cg * 8 + k] = a[1][k]; }
  __syncthreads();
  {
    const int b = tid >> 7, n = tid & 127;
    float s = 0.f;
#pragma unroll
    for (int g = 0; g < 16; ++g) s += red[(g * NB + b) * 128 + n];
    ws[WS_Y + ((size_t)h * NB + b) * DM + cq * 128 + n] = s;
  }
}

__global__ void __launch_bounds__(256) k_gu(const unsigned short* __restrict__ x, const unsigned short* __restrict__ wg,
                                            const unsigned short* __restrict__ wu, float* __restrict__ ws) {
  __shared__ __attribute__((aligned(16))) float hs[NB * DM];
  __shared__ __attribute__((aligned(16))) float ln[NB * DM];
  __shared__ __attribute__((aligned(16))) float red[4 * 64];
  const int tid = threadIdx.x, lane = tid & 63, wave = tid >> 6;
  const int c0 = blockIdx.x * 16;
  const int ch = tid & 1, rg = tid >> 1;
  u32x4 g[4], u[4];
#pragma unroll
  for (int i = 0; i < 4; ++i) {
    g[i] = *(const u32x4*)(wg + (size_t)(rg + 128 * i) * FF + c0 + ch * 8);
    u[i] = *(const u32x4*)(wu + (size_t)(rg + 128 * i) * FF + c0 + ch * 8);
  }
#pragma unroll
  for (int i = 0; i < 4; ++i) {
    const int e = tid + 256 * i;
    float s = 0.f;
#pragma unroll 8
    for (int hh = 0; hh < NH; ++hh) s += ws[WS_Y + (size_t)hh * NB * DM + e];
    const float hv = rbf(bf2f(x[e]) + rbf(s));
    hs[e] = hv;
    if (blockIdx.x == 0) ws[WS_H + e] = hv;
  }
  __syncthreads();
  if (wave < 2) ln512_wave(hs + wave * DM, ln + wave * DM, lane);
  __syncthreads();
  float a[4][8];
#pragma unroll
  for (int q = 0; q < 4; ++q)
#pragma unroll
    for (int k = 0; k < 8; ++k) a[q][k] = 0.f;
#pragma unroll
  for (int i = 0; i < 4; ++i) {
    const float x0 = ln[rg + 128 * i], x1 = ln[DM + rg + 128 * i];
#pragma unroll
    for (int k = 0; k < 4; ++k) {
      a[0][2 * k] = fmaf(x0, lo16(g[i][k]), a[0][2 * k]); a[0][2 * k + 1] = fmaf(x0, hi16(g[i][k]), a[0][2 * k + 1]);
      a[1][2 * k] = fmaf(x0, lo16(u[i][k]), a[1][2 * k]); a[1][2 * k + 1] = fmaf(x0, hi16(u[i][k]), a[1][2 * k + 1]);
      a[2][2 * k] = fmaf(x1, lo16(g[i][k]), a[2][2 * k]); a[2][2 * k + 1] = fmaf(x1, hi16(g[i][k]), a[2][2 * k + 1]);
      a[3][2 * k] = fmaf(x1, lo16(u[i][k]), a[3][2 * k]); a[3][2 * k + 1] = fmaf(x1, hi16(u[i][k]), a[3][2 * k + 1]);
    }
  }
#pragma unroll
  for (int q = 0; q < 4; ++q)
#pragma unroll
    for (int k = 0; k < 8; ++k) {
      float v = a[q][k];
      v += __shfl_xor(v, 2); v += __shfl_xor(v, 4); v += __shfl_xor(v, 8); v += __shfl_xor(v, 16); v += __shfl_xor(v, 32);
      a[q][k] = v;
    }
  if (lane < 2) {
#pragma unroll
    for (int q = 0; q < 4; ++q)
#pragma unroll
      for (int k = 0; k < 8; ++k) red[wave * 64 + q * 16 + lane * 8 + k] = a[q][k];
  }
  __syncthreads();
  if (tid < 32) {
    const int b = tid >> 4, cc = tid & 15;
    float gs = 0.f, us = 0.f;
#pragma unroll
    for (int w = 0; w < 4; ++w) { gs += red[w * 64 + (b * 2) * 16 + cc]; us += red[w * 64 + (b * 2 + 1) * 16 + cc]; }
    const float gb = rbf(gs), ub = rbf(us);
    const float sg = rbf(gb / (1.0f + __expf(-gb)));
    ws[WS_ACT + b * FF + c0 + cc] = rbf(sg * ub);
  }
}

__global__ void __launch_bounds__(256) k_down(const unsigned short* __restrict__ wd, const float* __restrict__ ws, unsigned short* __restrict__ out) {
  __shared__ __attribute__((aligned(16))) float red[4 * 32];
  const int tid = threadIdx.x, lane = tid & 63, wave = tid >> 6;
  const int c0 = blockIdx.x * 16;
  const int ch = tid & 1, rg = tid >> 1;
  float a[2][8];
#pragma unroll
  for (int k = 0; k < 8; ++k) { a[0][k] = 0.f; a[1][k] = 0.f; }
  u32x4 w[14];
#pragma unroll
  for (int i = 0; i < 14; ++i) w[i] = *(const u32x4*)(wd + (size_t)(rg + 128 * i) * DM + c0 + ch * 8);
#pragma unroll
  for (int i = 0; i < 14; ++i) {
    const float a0 = ws[WS_ACT + rg + 128 * i], a1 = ws[WS_ACT + FF + rg + 128 * i];
#pragma unroll
    for (int k = 0; k < 4; ++k) {
      a[0][2 * k] = fmaf(a0, lo16(w[i][k]), a[0][2 * k]); a[0][2 * k + 1] = fmaf(a0, hi16(w[i][k]), a[0][2 * k + 1]);
      a[1][2 * k] = fmaf(a1, lo16(w[i][k]), a[1][2 * k]); a[1][2 * k + 1] = fmaf(a1, hi16(w[i][k]), a[1][2 * k + 1]);
    }
  }
#pragma unroll
  for (int q = 0; q < 2; ++q)
#pragma unroll
    for (int k = 0; k < 8; ++k) {
      float v = a[q][k];
      v += __shfl_xor(v, 2); v += __shfl_xor(v, 4); v += __shfl_xor(v, 8); v += __shfl_xor(v, 16); v += __shfl_xor(v, 32);
      a[q][k] = v;
    }
  if (lane < 2) {
#pragma unroll
    for (int q = 0; q < 2; ++q)
#pragma unroll
      for (int k = 0; k < 8; ++k) red[wave * 32 + q * 16 + lane * 8 + k] = a[q][k];
  }
  __syncthreads();
  if (tid < 32) {
    const int b = tid >> 4, cc = tid & 15;
    float s = 0.f;
#pragma unroll
    for (int w2 = 0; w2 < 4; ++w2) s += red[w2 * 32 + b * 16 + cc];
    out[b * DM + c0 + cc] = f2bf(ws[WS_H + b * DM + c0 + cc] + rbf(s));
  }
}

extern "C" void kernel_launch(void* const* d_in, const int* in_sizes, int n_in,
                              void* d_out, int out_size, void* d_ws, size_t ws_size,
                              hipStream_t stream) {
  const unsigned short* x = (const unsigned short*)d_in[0];
  const unsigned short* kc = (const unsigned short*)d_in[1];
  const unsigned short* vc = (const unsigned short*)d_in[2];
  const unsigned short* wq = (const unsigned short*)d_in[3];
  const unsigned short* wo = (const unsigned short*)d_in[4];
  const unsigned short* wg = (const unsigned short*)d_in[6];
  const unsigned short* wu = (const unsigned short*)d_in[7];
  const unsigned short* wd = (const unsigned short*)d_in[8];
  float* ws = (float*)d_ws;
  k_attn<<<256, NTHR, 0, stream>>>(x, kc, vc, wq, ws);
  k_wo<<<128, 256, 0, stream>>>(wo, ws);
  k_gu<<<112, 256, 0, stream>>>(x, wg, wu, ws);
  k_down<<<32, 256, 0, stream>>>(wd, ws, (unsigned short*)d_out);
}
```

```cpp
#include <hip/hip_runtime.h>
#include <hip/hip_bf16.h>
#include <stdint.h>

typedef unsigned u32x4 __attribute__((ext_vector_type(4)));
typedef float f32x4 __attribute__((ext_vector_type(4)));
typedef __bf16 bf16x2 __attribute__((ext_vector_type(2)));
typedef unsigned long long u64;

constexpr int NB = 2, DM = 512, NH = 32, SK = 16384, DH = 128, FF = 1792;
constexpr int NSPLIT = 4;
constexpr int KPS = SK / NSPLIT;
constexpr int NWAVE = 8, NTHR = 512;
constexpr int PAGE = 16;
constexpr int NPAGE = KPS / NWAVE / PAGE;
constexpr int NPART = NB * NH * NSPLIT;

__device__ __forceinline__ float bf2f(unsigned short b) { return __builtin_bit_cast(float, (unsigned)b << 16); }
__device__ __forceinline__ float rbf(float x) { return __bfloat162float(__float2bfloat16(x)); }
__device__ __forceinline__ unsigned short f2bf(float x) { return __builtin_bit_cast(unsigned short, __float2bfloat16(x)); }
__device__ __forceinline__ float lo16(unsigned u) { return __builtin_bit_cast(float, u << 16); }
__device__ __forceinline__ float hi16(unsigned u) { return __builtin_bit_cast(float, u & 0xffff0000u); }
__device__ __forceinline__ float dot2(unsigned k, unsigned q, float acc) {
  return __builtin_amdgcn_fdot2_f32_bf16(__builtin_bit_cast(bf16x2, k), __builtin_bit_cast(bf16x2, q), acc, false);
}
template <int CTRL> __device__ __forceinline__ float dpp(float x) {
  return __builtin_bit_cast(float, __builtin_amdgcn_mov_dpp(__builtin_bit_cast(int, x), CTRL, 0xf, 0xf, true));
}
constexpr int XOR1 = 0xB1, XOR2 = 0x4E, XOR7 = 0x141, XOR8 = 0x128;
__device__ __forceinline__ float row16_sum(float x) {
  x += dpp<XOR1>(x); x += dpp<XOR2>(x); x += dpp<XOR7>(x); x += dpp<XOR8>(x); return x;
}
__device__ __forceinline__ float xrow_sum(float x) {
  auto s = __builtin_amdgcn_permlane16_swap(__float_as_uint(x), __float_as_uint(x), false, false);
  x = __uint_as_float(s[0]) + __uint_as_float(s[1]);
  auto t = __builtin_amdgcn_permlane32_swap(__float_as_uint(x), __float_as_uint(x), false, false);
  return __uint_as_float(t[0]) + __uint_as_float(t[1]);
}
__device__ __forceinline__ float wave_sum(float x) { return xrow_sum(row16_sum(x)); }

constexpr size_t WS_G1 = 0;
constexpr size_t WS_G2 = 4096;
constexpr size_t WS_GEND = WS_G2 + FF * 8;
constexpr size_t WS_PML = 32768;
constexpr size_t WS_Y = 65536;

constexpr int L_WO = 0, L_RED = 131072, L_XS = L_RED + 32 * 132 * 4, L_LN = L_XS + 2048, L_Q = L_LN + 2048, L_O = L_Q + 256, L_END = L_O + 512;

__device__ __forceinline__ void glds16(const void* gsrc, unsigned lds_dst) {
  unsigned keep;
  asm volatile("s_mov_b32 %0, m0\n\ts_mov_b32 m0, %2\n\ts_nop 0\n\tglobal_load_lds_dwordx4 %1, off\n\ts_mov_b32 m0, %0" : "=&s"(keep) : "v"(gsrc), "s"(lds_dst) : "memory");
}

__global__ void __launch_bounds__(NTHR, 2) k_attn(const unsigned short* __restrict__ x, const unsigned short* __restrict__ kc,
                                                  const unsigned short* __restrict__ vc, const unsigned short* __restrict__ wq,
                                                  const unsigned short* __restrict__ wo, unsigned char* __restrict__ ws) {
  __shared__ __attribute__((aligned(16))) unsigned char smem[L_END];
  float* red = (float*)(smem + L_RED);
  float* xs = (float*)(smem + L_XS);
  float* lnx = (float*)(smem + L_LN);
  unsigned* qs = (unsigned*)(smem + L_Q);
  float* os = (float*)(smem + L_O);
  const int tid = threadIdx.x, lane = tid & 63, wave = __builtin_amdgcn_readfirstlane(tid >> 6), r = lane >> 4, c = lane & 15;
  const int bid = blockIdx.x, xcd = bid & 7, j = bid >> 3;
  const int h = xcd * 4 + (j >> 3), b = (j >> 2) & 1, sp = j & 3;
  const unsigned short xv = x[b * DM + tid];
  const int cg = tid & 15, rg = tid >> 4;
  u32x4 w[16];
  {
    const u32x4* wp = (const u32x4*)(wq + (size_t)h * DH) + cg;
#pragma unroll
    for (int i = 0; i < 16; ++i) w[i] = wp[(size_t)(rg + 32 * i) * (NH * DH / 8)];
  }
  const size_t base = ((size_t)(b * NH + h) * SK + (size_t)sp * KPS) * DH;
  const u32x4* kp = (const u32x4*)(kc + base) + wave * 256 + r * 16 + c;
  const u32x4* vp = (const u32x4*)(vc + base) + wave * 256 + r * 16 + c;
  constexpr int DEPTH = 4;
  u32x4 Kb[DEPTH][4], Vb[DEPTH][4];
#pragma unroll
  for (int d = 0; d < DEPTH - 1; ++d)
#pragma unroll
    for (int t = 0; t < 4; ++t) {
      Kb[d][t] = __builtin_nontemporal_load(kp + d * 2048 + t * 64);
      Vb[d][t] = __builtin_nontemporal_load(vp + d * 2048 + t * 64);
    }
  __builtin_amdgcn_sched_barrier(0);
  if (bid == 0) {
    for (int i = tid; i < (int)(WS_GEND / 16); i += NTHR) ((u32x4*)ws)[i] = u32x4{0u, 0u, 0u, 0u};
  }
  xs[tid] = bf2f(xv);
  __syncthreads();
  if (wave == 0) {
    float v[8]; float s = 0.f;
#pragma unroll
    for (int i = 0; i < 8; ++i) { v[i] = xs[lane + 64 * i]; s += v[i]; }
    s = wave_sum(s);
    const float mean = s * (1.0f / 512.0f);
    float ss = 0.f;
#pragma unroll
    for (int i = 0; i < 8; ++i) { const float d = v[i] - mean; ss += d * d; }
    ss = wave_sum(ss);
    const float var = rbf(ss * (1.0f / 512.0f)), mb = rbf(mean);
    const float den = rbf(sqrtf(rbf(var + 1e-5f)));
#pragma unroll
    for (int i = 0; i < 8; ++i) lnx[lane + 64 * i] = rbf(rbf(v[i] - mb) / den);
  }
  __syncthreads();
  {
    float a[8];
#pragma unroll
    for (int k = 0; k < 8; ++k) a[k] = 0.f;
#pragma unroll
    for (int i = 0; i < 16; ++i) {
      const float xl = lnx[rg + 32 * i];
#pragma unroll
      for (int k = 0; k < 4; ++k) { a[2 * k] = fmaf(xl, lo16(w[i][k]), a[2 * k]); a[2 * k + 1] = fmaf(xl, hi16(w[i][k]), a[2 * k + 1]); }
    }
#pragma unroll
    for (int k = 0; k < 8; ++k) red[rg * 132 + cg * 8 + k] = a[k];
  }
  __syncthreads();
  if (tid < 64) {
    float s0 = 0.f, s1 = 0.f;
#pragma unroll
    for (int g = 0; g < 32; ++g) { s0 += red[g * 132 + 2 * tid]; s1 += red[g * 132 + 2 * tid + 1]; }
    qs[tid] = (unsigned)f2bf(s0) | ((unsigned)f2bf(s1) << 16);
  }
  __syncthreads();
  const u32x4 q = *(const u32x4*)(qs + 4 * c);
  {
    const unsigned char* src = (const unsigned char*)(wo + ((size_t)h * DH + wave * 16) * DM) + lane * 16;
    const unsigned dst = (unsigned)(uintptr_t)((__attribute__((address_space(3))) unsigned char*)(smem + L_WO)) + wave * 16 * 1024;
#pragma unroll
    for (int i = 0; i < 16; ++i) glds16(src + i * 1024, dst + i * 1024);
  }
  constexpr float SCALE = 0.08838834764831845f, L2E = 1.4426950408889634f;
  float m = -INFINITY, l = 0.f, acc[8];
#pragma unroll
  for (int k = 0; k < 8; ++k) acc[k] = 0.f;
  for (int p0 = 0; p0 < NPAGE; p0 += DEPTH) {
#pragma unroll
    for (int d = 0; d < DEPTH; ++d) {
      const int pn = p0 + d + DEPTH - 1;
      if (pn < NPAGE) {
#pragma unroll
        for (int t = 0; t < 4; ++t) {
          Kb[(d + DEPTH - 1) % DEPTH][t] = __builtin_nontemporal_load(kp + pn * 2048 + t * 64);
          Vb[(d + DEPTH - 1) % DEPTH][t] = __builtin_nontemporal_load(vp + pn * 2048 + t * 64);
        }
      }
      float s[4];
#pragma unroll
      for (int t = 0; t < 4; ++t) {
        float dd = 0.f;
#pragma unroll
        for (int k = 0; k < 4; ++k) dd = dot2(Kb[d][t][k], q[k], dd);
        dd = row16_sum(dd);
        s[t] = rbf(rbf(dd) * SCALE) * L2E;
      }
      const float pm = fmaxf(fmaxf(s[0], s[1]), fmaxf(s[2], s[3]));
      const float mn = fmaxf(m, pm);
      const float alpha = __builtin_amdgcn_exp2f(m - mn);
      m = mn;
      float pp[4];
#pragma unroll
      for (int t = 0; t < 4; ++t) pp[t] = __builtin_amdgcn_exp2f(s[t] - mn);
      l = fmaf(l, alpha, (pp[0] + pp[1]) + (pp[2] + pp[3]));
#pragma unroll
      for (int k = 0; k < 8; ++k) acc[k] *= alpha;
#pragma unroll
      for (int t = 0; t < 4; ++t)
#pragma unroll
        for (int k = 0; k < 4; ++k) {
          acc[2 * k] = fmaf(pp[t], lo16(Vb[d][t][k]), acc[2 * k]);
          acc[2 * k + 1] = fmaf(pp[t], hi16(Vb[d][t][k]), acc[2 * k + 1]);
        }
    }
  }
  {
    float* rec = red + (wave * 4 + r) * 132;
#pragma unroll
    for (int k = 0; k < 8; ++k) rec[4 + 8 * c + k] = acc[k];
    if (c == 0) { rec[0] = m; rec[1] = l; }
  }
  asm volatile("s_waitcnt vmcnt(0)" ::: "memory");
  __syncthreads();
  const int part = (b * NH + h) * NSPLIT + sp;
  if (tid < DH) {
    float M = -INFINITY;
#pragma unroll
    for (int g = 0; g < 32; ++g) M = fmaxf(M, red[g * 132]);
    float L = 0.f, o = 0.f;
#pragma unroll
    for (int g = 0; g < 32; ++g) {
      const float wgt = __builtin_amdgcn_exp2f(red[g * 132] - M);
      L = fmaf(wgt, red[g * 132 + 1], L);
      o = fmaf(wgt, red[g * 132 + 4 + tid], o);
    }
    os[tid] = o;
    if (tid == 0) { ((float2*)(ws + WS_PML))[part] = make_float2(M, L); }
  }
  __syncthreads();
  {
    float a[8];
#pragma unroll
    for (int k = 0; k < 8; ++k) a[k] = 0.f;
    const u32x4* wl = (const u32x4*)(smem + L_WO) + wave * 16 * 64 + lane;
#pragma unroll
    for (int i = 0; i < 16; ++i) {
      const u32x4 ww = wl[i * 64];
      const float ov = os[wave * 16 + i];
#pragma unroll
      for (int k = 0; k < 4; ++k) { a[2 * k] = fmaf(ov, lo16(ww[k]), a[2 * k]); a[2 * k + 1] = fmaf(ov, hi16(ww[k]), a[2 * k + 1]); }
    }
    *(f32x4*)(red + wave * 512 + lane * 8) = f32x4{a[0], a[1], a[2], a[3]};
    *(f32x4*)(red + wave * 512 + lane * 8 + 4) = f32x4{a[4], a[5], a[6], a[7]};
  }
  __syncthreads();
  {
    float s = 0.f;
#pragma unroll
    for (int g = 0; g < 8; ++g) s += red[g * 512 + tid];
    ((float*)(ws + WS_Y))[((size_t)(tid >> 3) * NPART + part) * 8 + (tid & 7)] = s;
  }
}

#define RLX_AGENT __ATOMIC_RELAXED, __HIP_MEMORY_SCOPE_AGENT
constexpr unsigned SPIN_CAP = 1u << 22;

__global__ void __launch_bounds__(256) k_tail(const unsigned short* __restrict__ x, const unsigned short* __restrict__ wg,
                                              const unsigned short* __restrict__ wu, const unsigned short* __restrict__ wd,
                                              unsigned char* ws, unsigned short* __restrict__ out) {
  __shared__ __attribute__((aligned(16))) float hs[NB * DM];
  __shared__ __attribute__((aligned(16))) float red[4 * 64];
  __shared__ __attribute__((aligned(16))) float hmine[16];
  const int tid = threadIdx.x, lane = tid & 63, wave = tid >> 6, j = blockIdx.x;
  const int ch = tid & 1, rg = tid >> 1;
  u64* g1 = (u64*)(ws + WS_G1);
  u64* g2 = (u64*)(ws + WS_G2);
  float2 ml = make_float2(0.f, 0.f); f32x4 y0 = {0, 0, 0, 0}, y1 = {0, 0, 0, 0};
  if (j < 64) {
    ml = ((const float2*)(ws + WS_PML))[tid];
    const f32x4* yp = (const f32x4*)(ws + WS_Y) + ((size_t)j * NPART + tid) * 2;
    y0 = yp[0]; y1 = yp[1];
  }
  u32x4 g[4], u[4], d[7];
#pragma unroll
  for (int i = 0; i < 4; ++i) {
    g[i] = *(const u32x4*)(wg + (size_t)(rg + 128 * i) * FF + j * 16 + ch * 8);
    u[i] = *(const u32x4*)(wu + (size_t)(rg + 128 * i) * FF + j * 16 + ch * 8);
  }
  if (j < 64) {
#pragma unroll
    for (int i = 0; i < 7; ++i) d[i] = *(const u32x4*)(wd + (size_t)(tid + 256 * i) * DM + j * 8);
  } else {
#pragma unroll
    for (int i = 0; i < 7; ++i) d[i] = u32x4{0u, 0u, 0u, 0u};
  }
  __builtin_amdgcn_sched_barrier(0);
  if (j < 64) {
    float M = fmaxf(ml.x, dpp<XOR1>(ml.x)); M = fmaxf(M, dpp<XOR2>(M));
    const float wgt = __builtin_amdgcn_exp2f(ml.x - M);
    float L = wgt * ml.y; L += dpp<XOR1>(L); L += dpp<XOR2>(L);
    const float cf = wgt / L;
    float v[8] = {y0[0] * cf, y0[1] * cf, y0[2] * cf, y0[3] * cf, y1[0] * cf, y1[1] * cf, y1[2] * cf, y1[3] * cf};
#pragma unroll
    for (int k = 0; k < 8; ++k) v[k] = wave_sum(v[k]);
    if (lane == 0) {
#pragma unroll
      for (int k = 0; k < 8; ++k) red[wave * 8 + k] = v[k];
    }
    __syncthreads();
    if (tid < 8) {
      const float s0 = red[tid] + red[8 + tid], s1 = red[16 + tid] + red[24 + tid];
      const unsigned short h0 = f2bf(bf2f(x[j * 8 + tid]) + rbf(s0)), h1 = f2bf(bf2f(x[DM + j * 8 + tid]) + rbf(s1));
      hmine[tid] = bf2f(h0); hmine[8 + tid] = bf2f(h1);
      __hip_atomic_store(g1 + j * 8 + tid, (1ull << 32) | ((unsigned)h1 << 16) | (unsigned)h0, RLX_AGENT);
    }
  }
  {
    u64 a0, a1; unsigned spins = 0;
    do {
      a0 = __hip_atomic_load(g1 + tid, RLX_AGENT);
      a1 = __hip_atomic_load(g1 + 256 + tid, RLX_AGENT);
    } while ((((a0 & a1) >> 32) != 1ull) && ++spins < SPIN_CAP);
    hs[tid] = lo16((unsigned)a0); hs[DM + tid] = hi16((unsigned)a0);
    hs[256 + tid] = lo16((unsigned)a1); hs[DM + 256 + tid] = hi16((unsigned)a1);
  }
  __syncthreads();
  float mb0, mb1, den0, den1;
  {
    float v0[8], v1[8], s0 = 0.f, s1 = 0.f;
#pragma unroll
    for (int i = 0; i < 8; ++i) { v0[i] = hs[lane + 64 * i]; v1[i] = hs[DM + lane + 64 * i]; s0 += v0[i]; s1 += v1[i]; }
    s0 = wave_sum(s0); s1 = wave_sum(s1);
    const float mean0 = s0 * (1.0f / 512.0f), mean1 = s1 * (1.0f / 512.0f);
    float q0 = 0.f, q1 = 0.f;
#pragma unroll
    for (int i = 0; i < 8; ++i) { const float d0 = v0[i] - mean0, d1 = v1[i] - mean1; q0 += d0 * d0; q1 += d1 * d1; }
    q0 = wave_sum(q0); q1 = wave_sum(q1);
    mb0 = rbf(mean0); mb1 = rbf(mean1);
    den0 = rbf(sqrtf(rbf(rbf(q0 * (1.0f / 512.0f)) + 1e-5f)));
    den1 = rbf(sqrtf(rbf(rbf(q1 * (1.0f / 512.0f)) + 1e-5f)));
  }
  {
    float a[4][8];
#pragma unroll
    for (int qq = 0; qq < 4; ++qq)
#pragma unroll
      for (int k = 0; k < 8; ++k) a[qq][k] = 0.f;
#pragma unroll
    for (int i = 0; i < 4; ++i) {
      const float x0 = rbf(rbf(hs[rg + 128 * i] - mb0) / den0), x1 = rbf(rbf(hs[DM + rg + 128 * i] - mb1) / den1);
#pragma unroll
      for (int k = 0; k < 4; ++k) {
        a[0][2 * k] = fmaf(x0, lo16(g[i][k]), a[0][2 * k]); a[0][2 * k + 1] = fmaf(x0, hi16(g[i][k]), a[0][2 * k + 1]);
        a[1][2 * k] = fmaf(x0, lo16(u[i][k]), a[1][2 * k]); a[1][2 * k + 1] = fmaf(x0, hi16(u[i][k]), a[1][2 * k + 1]);
        a[2][2 * k] = fmaf(x1, lo16(g[i][k]), a[2][2 * k]); a[2][2 * k + 1] = fmaf(x1, hi16(g[i][k]), a[2][2 * k + 1]);
        a[3][2 * k] = fmaf(x1, lo16(u[i][k]), a[3][2 * k]); a[3][2 * k + 1] = fmaf(x1, hi16(u[i][k]), a[3][2 * k + 1]);
      }
    }
#pragma unroll
    for (int qq = 0; qq < 4; ++qq)
#pragma unroll
      for (int k = 0; k < 8; ++k) {
        float v = a[qq][k];
        v += dpp<XOR2>(v); v += dpp<0x124>(v)  ; v += dpp<XOR8>(v);
        a[qq][k] = xrow_sum(v);
      }
    __syncthreads();
    if (lane < 2) {
#pragma unroll
      for (int qq = 0; qq < 4; ++qq)
#pragma unroll
        for (int k = 0; k < 8; ++k) red[wave * 64 + qq * 16 + lane * 8 + k] = a[qq][k];
    }
    __syncthreads();
    if (tid < 16) {
      float g0 = 0.f, u0 = 0.f, g1v = 0.f, u1 = 0.f;
#pragma unroll
      for (int w2 = 0; w2 < 4; ++w2) { g0 += red[w2 * 64 + tid]; u0 += red[w2 * 64 + 16 + tid]; g1v += red[w2 * 64 + 32 + tid]; u1 += red[w2 * 64 + 48 + tid]; }
      g0 = rbf(g0); u0 = rbf(u0); g1v = rbf(g1v); u1 = rbf(u1);
      const unsigned short a0 = f2bf(rbf(g0 / (1.0f + __expf(-g0))) * u0), a1 = f2bf(rbf(g1v / (1.0f + __expf(-g1v))) * u1);
      __hip_atomic_store(g2 + j * 16 + tid, (1ull << 32) | ((unsigned)a1 << 16) | (unsigned)a0, RLX_AGENT);
    }
  }
  if (j >= 64) return;
  {
    u64 av[7]; unsigned spins = 0; bool ok;
    do {
      ok = true;
#pragma unroll
      for (int i = 0; i < 7; ++i) { av[i] = __hip_atomic_load(g2 + tid + 256 * i, RLX_AGENT); }
#pragma unroll
      for (int i = 0; i < 7; ++i) ok &= (av[i] >> 32) == 1ull;
    } while (!ok && ++spins < SPIN_CAP);
    float a[2][8];
#pragma unroll
    for (int k = 0; k < 8; ++k) { a[0][k] = 0.f; a[1][k] = 0.f; }
#pragma unroll
    for (int i = 0; i < 7; ++i) {
      const float a0 = lo16((unsigned)av[i]), a1 = hi16((unsigned)av[i]);
#pragma unroll
      for (int k = 0; k < 4; ++k) {
        a[0][2 * k] = fmaf(a0, lo16(d[i][k]), a[0][2 * k]); a[0][2 * k + 1] = fmaf(a0, hi16(d[i][k]), a[0][2 * k + 1]);
        a[1][2 * k] = fmaf(a1, lo16(d[i][k]), a[1][2 * k]); a[1][2 * k + 1] = fmaf(a1, hi16(d[i][k]), a[1][2 * k + 1]);
      }
    }
#pragma unroll
    for (int qq = 0; qq < 2; ++qq)
#pragma unroll
      for (int k = 0; k < 8; ++k) a[qq][k] = wave_sum(a[qq][k]);
    __syncthreads();
    if (lane == 0) {
#pragma unroll
      for (int qq = 0; qq < 2; ++qq)
#pragma unroll
        for (int k = 0; k < 8; ++k) red[wave * 16 + qq * 8 + k] = a[qq][k];
    }
    __syncthreads();
    if (tid < 16) {
      const float s = (red[tid] + red[16 + tid]) + (red[32 + tid] + red[48 + tid]);
      out[(tid >> 3) * DM + j * 8 + (tid & 7)] = f2bf(hmine[tid] + rbf(s));
    }
  }
}

extern "C" void kernel_launch(void* const* d_in, const int* in_sizes, int n_in,
                              void* d_out, int out_size, void* d_ws, size_t ws_size,
                              hipStream_t stream) {
  const unsigned short* x = (const unsigned short*)d_in[0];
  const unsigned short* kc = (const unsigned short*)d_in[1];
  const unsigned short* vc = (const unsigned short*)d_in[2];
  const unsigned short* wq = (const unsigned short*)d_in[3];
  const unsigned short* wo = (const unsigned short*)d_in[4];
  const unsigned short* wg = (const unsigned short*)d_in[6];
  const unsigned short* wu = (const unsigned short*)d_in[7];
  const unsigned short* wd = (const unsigned short*)d_in[8];
  unsigned char* ws = (unsigned char*)d_ws;
  k_attn<<<256, NTHR, 0, stream>>>(x, kc, vc, wq, wo, ws);
  k_tail<<<112, 256, 0, stream>>>(x, wg, wu, wd, ws, (unsigned short*)d_out);
}
```

```cpp
#include <hip/hip_runtime.h>
#include <hip/hip_bf16.h>
#include <stdint.h>

typedef unsigned u32x4 __attribute__((ext_vector_type(4)));
typedef float f32x4 __attribute__((ext_vector_type(4)));
typedef __bf16 bf16x2 __attribute__((ext_vector_type(2)));
typedef unsigned long long u64;

constexpr int NB = 2, DM = 512, NH = 32, SK = 16384, DH = 128, FF = 1792;
constexpr int NSPLIT = 4;
constexpr int KPS = SK / NSPLIT;
constexpr int NWAVE = 8, NTHR = 512;
constexpr int PAGE = 16;
constexpr int NPAGE = KPS / NWAVE / PAGE;
constexpr int NPART = NB * NH * NSPLIT;

__device__ __forceinline__ float bf2f(unsigned short b) { return __builtin_bit_cast(float, (unsigned)b << 16); }
__device__ __forceinline__ float rbf(float x) { return __bfloat162float(__float2bfloat16(x)); }
__device__ __forceinline__ unsigned short f2bf(float x) { return __builtin_bit_cast(unsigned short, __float2bfloat16(x)); }
__device__ __forceinline__ float lo16(unsigned u) { return __builtin_bit_cast(float, u << 16); }
__device__ __forceinline__ float hi16(unsigned u) { return __builtin_bit_cast(float, u & 0xffff0000u); }
__device__ __forceinline__ float dot2(unsigned k, unsigned q, float acc) {
  return __builtin_amdgcn_fdot2_f32_bf16(__builtin_bit_cast(bf16x2, k), __builtin_bit_cast(bf16x2, q), acc, false);
}
template <int CTRL> __device__ __forceinline__ float dpp(float x) {
  return __builtin_bit_cast(float, __builtin_amdgcn_mov_dpp(__builtin_bit_cast(int, x), CTRL, 0xf, 0xf, true));
}
constexpr int XOR1 = 0xB1, XOR2 = 0x4E, XOR7 = 0x141, XOR8 = 0x128;
__device__ __forceinline__ float row16_sum(float x) {
  x += dpp<XOR1>(x); x += dpp<XOR2>(x); x += dpp<XOR7>(x); x += dpp<XOR8>(x); return x;
}
__device__ __forceinline__ float xrow_sum(float x) {
  auto s = __builtin_amdgcn_permlane16_swap(__float_as_uint(x), __float_as_uint(x), false, false);
  x = __uint_as_float(s[0]) + __uint_as_float(s[1]);
  auto t = __builtin_amdgcn_permlane32_swap(__float_as_uint(x), __float_as_uint(x), false, false);
  return __uint_as_float(t[0]) + __uint_as_float(t[1]);
}
__device__ __forceinline__ float wave_sum(float x) { return xrow_sum(row16_sum(x)); }

#define STAMP(k, i)
constexpr size_t WS_G1 = 0;
constexpr size_t WS_G2 = 4096;
constexpr size_t WS_GEND = WS_G2 + FF * 8;
constexpr size_t WS_PML = 32768;
constexpr size_t WS_Y = 65536;
constexpr size_t WS_GU = 1u << 20;
constexpr size_t WS_DT = 5u << 20;

constexpr int L_WO = 0, L_RED = 131072, L_XS = L_RED + 32 * 132 * 4, L_LN = L_XS + 2048, L_Q = L_LN + 2048, L_O = L_Q + 256, L_CTR = L_O + 512, L_END = L_CTR + 16;

__device__ __forceinline__ void glds16(const void* gsrc, unsigned lds_dst) {
  unsigned keep;
  asm volatile("s_mov_b32 %0, m0\n\ts_mov_b32 m0, %2\n\ts_nop 0\n\tglobal_load_lds_dwordx4 %1, off\n\ts_mov_b32 m0, %0" : "=&s"(keep) : "v"(gsrc), "s"(lds_dst) : "memory");
}

__device__ __forceinline__ int take_ticket(unsigned lds_addr) {
  int tk; unsigned long long save;
  asm volatile("s_mov_b64 %1, exec\n\ts_mov_b64 exec, 1\n\tds_add_rtn_u32 %0, %2, %3\n\ts_mov_b64 exec, %1"
               : "=&v"(tk), "=&s"(save) : "v"(lds_addr), "v"(1u) : "memory");
  return tk;
}

__global__ void __launch_bounds__(NTHR, 2) k_attn(const unsigned short* __restrict__ x, const unsigned short* __restrict__ kc,
                                                  const unsigned short* __restrict__ vc, const unsigned short* __restrict__ wq,
                                                  const unsigned short* __restrict__ wo, const unsigned short* __restrict__ wg,
                                                  const unsigned short* __restrict__ wu, const unsigned short* __restrict__ wd,
                                                  unsigned char* __restrict__ ws) {
  __shared__ __attribute__((aligned(16))) unsigned char smem[L_END];
  float* red = (float*)(smem + L_RED);
  float* xs = (float*)(smem + L_XS);
  float* lnx = (float*)(smem + L_LN);
  unsigned* qs = (unsigned*)(smem + L_Q);
  float* os = (float*)(smem + L_O);
  const int tid = threadIdx.x, lane = tid & 63, wave = __builtin_amdgcn_readfirstlane(tid >> 6), r = lane >> 4, c = lane & 15;
  const int bid = blockIdx.x, xcd = bid & 7, j = bid >> 3;
  const int h = xcd * 4 + (j >> 3), b = (j >> 2) & 1, sp = j & 3;
  STAMP(0, 0);
  const unsigned short xv = x[b * DM + tid];
  const int cg = tid & 15, rg = tid >> 4;
  u32x4 w[16];
  {
    const u32x4* wp = (const u32x4*)(wq + (size_t)h * DH) + cg;
#pragma unroll
    for (int i = 0; i < 16; ++i) w[i] = wp[(size_t)(rg + 32 * i) * (NH * DH / 8)];
  }
  u32x4 tl[4];
  const bool job_gu = bid < 112, job_d = bid >= 128 && bid < 192;
  if (job_gu) {
    const int t = tid & 255;
    const unsigned short* s = ((tid >> 8) ? wu : wg) + (size_t)(t >> 1) * FF + bid * 16 + (t & 1) * 8;
#pragma unroll
    for (int i = 0; i < 4; ++i) tl[i] = *(const u32x4*)(s + (size_t)128 * i * FF);
  } else if (job_d) {
    const unsigned short* s = wd + (size_t)tid * DM + (bid - 128) * 8;
#pragma unroll
    for (int i = 0; i < 4; ++i) tl[i] = *(const u32x4*)(s + (size_t)((i < 3 || tid < 256) ? 512 * i : 0) * DM);
  }
  __builtin_amdgcn_sched_barrier(0);
  __builtin_amdgcn_s_barrier();
  __builtin_amdgcn_sched_barrier(0);
  const size_t base = ((size_t)(b * NH + h) * SK + (size_t)sp * KPS) * DH;
  const u32x4* kp = (const u32x4*)(kc + base) + r * 16 + c;
  const u32x4* vp = (const u32x4*)(vc + base) + r * 16 + c;
  constexpr int DEPTH = 4;
  constexpr int NPG = KPS / PAGE;
  u32x4 Kb[DEPTH][4], Vb[DEPTH][4];
#pragma unroll
  for (int d = 0; d < DEPTH - 1; ++d)
#pragma unroll
    for (int t = 0; t < 4; ++t) {
      Kb[d][t] = __builtin_nontemporal_load(kp + (d * 8 + wave) * 256 + t * 64);
      Vb[d][t] = __builtin_nontemporal_load(vp + (d * 8 + wave) * 256 + t * 64);
    }
  __builtin_amdgcn_sched_barrier(0);
  unsigned* ctr = (unsigned*)(smem + L_CTR);
  if (tid == 0) *ctr = DEPTH * 8;
  if (bid == 0) {
    for (int i = tid; i < (int)(WS_GEND / 16); i += NTHR) ((u32x4*)ws)[i] = u32x4{0u, 0u, 0u, 0u};
  }
  xs[tid] = bf2f(xv);
  __syncthreads();
  if (wave == 0) {
    float v[8]; float s = 0.f;
#pragma unroll
    for (int i = 0; i < 8; ++i) { v[i] = xs[lane + 64 * i]; s += v[i]; }
    s = wave_sum(s);
    const float mean = s * (1.0f / 512.0f);
    float ss = 0.f;
#pragma unroll
    for (int i = 0; i < 8; ++i) { const float d = v[i] - mean; ss += d * d; }
    ss = wave_sum(ss);
    const float var = rbf(ss * (1.0f / 512.0f)), mb = rbf(mean);
    const float den = rbf(sqrtf(rbf(var + 1e-5f)));
#pragma unroll
    for (int i = 0; i < 8; ++i) lnx[lane + 64 * i] = rbf(rbf(v[i] - mb) / den);
  }
  __syncthreads();
  {
    float a[8];
#pragma unroll
    for (int k = 0; k < 8; ++k) a[k] = 0.f;
#pragma unroll
    for (int i = 0; i < 16; ++i) {
      const float xl = lnx[rg + 32 * i];
#pragma unroll
      for (int k = 0; k < 4; ++k) { a[2 * k] = fmaf(xl, lo16(w[i][k]), a[2 * k]); a[2 * k + 1] = fmaf(xl, hi16(w[i][k]), a[2 * k + 1]); }
    }
#pragma unroll
    for (int k = 0; k < 8; ++k) red[rg * 132 + cg * 8 + k] = a[k];
  }
  __syncthreads();
  if (tid < 64) {
    float s0 = 0.f, s1 = 0.f;
#pragma unroll
    for (int g = 0; g < 32; ++g) { s0 += red[g * 132 + 2 * tid]; s1 += red[g * 132 + 2 * tid + 1]; }
    qs[tid] = (unsigned)f2bf(s0) | ((unsigned)f2bf(s1) << 16);
  }
  __syncthreads();
  const u32x4 q = *(const u32x4*)(qs + 4 * c);
  STAMP(0, 1);
#pragma unroll
  for (int t = 0; t < 4; ++t) {
    Kb[DEPTH - 1][t] = __builtin_nontemporal_load(kp + ((DEPTH - 1) * 8 + wave) * 256 + t * 64);
    Vb[DEPTH - 1][t] = __builtin_nontemporal_load(vp + ((DEPTH - 1) * 8 + wave) * 256 + t * 64);
  }
  if (job_gu) {
    u32x4* dd = (u32x4*)(ws + WS_GU + (size_t)bid * 32768) + tid;
#pragma unroll
    for (int i = 0; i < 4; ++i) dd[i * 512] = tl[i];
  } else if (job_d) {
    u32x4* dd = (u32x4*)(ws + WS_DT + (size_t)(bid - 128) * 28672) + tid;
#pragma unroll
    for (int i = 0; i < 3; ++i) dd[i * 512] = tl[i];
    if (tid < 256) dd[3 * 512] = tl[3];
  }
  {
    const unsigned char* src = (const unsigned char*)(wo + ((size_t)h * DH + wave * 16) * DM) + lane * 16;
    const unsigned dst = (unsigned)(uintptr_t)((__attribute__((address_space(3))) unsigned char*)(smem + L_WO)) + wave * 16 * 1024;
#pragma unroll
    for (int i = 0; i < 16; ++i) glds16(src + i * 1024, dst + i * 1024);
  }
  constexpr float SCALE = 0.08838834764831845f, L2E = 1.4426950408889634f;
  float m = -INFINITY, l = 0.f, acc[8];
#pragma unroll
  for (int k = 0; k < 8; ++k) acc[k] = 0.f;
  const unsigned ctr_addr = (unsigned)(uintptr_t)((__attribute__((address_space(3))) unsigned char*)(smem + L_CTR));
  int tk = take_ticket(ctr_addr);
  bool live[DEPTH];
#pragma unroll
  for (int d = 0; d < DEPTH; ++d) live[d] = true;
  do {
#pragma unroll
    for (int d = 0; d < DEPTH; ++d) {
      float s[4];
#pragma unroll
      for (int t = 0; t < 4; ++t) {
        float dd = 0.f;
#pragma unroll
        for (int k = 0; k < 4; ++k) dd = dot2(Kb[d][t][k], q[k], dd);
        dd = row16_sum(dd);
        s[t] = live[d] ? rbf(rbf(dd) * SCALE) * L2E : -INFINITY;
      }
      const float pm = fmaxf(fmaxf(s[0], s[1]), fmaxf(s[2], s[3]));
      const float mn = fmaxf(m, pm);
      const float alpha = __builtin_amdgcn_exp2f(m - mn);
      m = mn;
      float pp[4];
#pragma unroll
      for (int t = 0; t < 4; ++t) pp[t] = __builtin_amdgcn_exp2f(s[t] - mn);
      l = fmaf(l, alpha, (pp[0] + pp[1]) + (pp[2] + pp[3]));
#pragma unroll
      for (int k = 0; k < 8; ++k) acc[k] *= alpha;
#pragma unroll
      for (int t = 0; t < 4; ++t)
#pragma unroll
        for (int k = 0; k < 4; ++k) {
          acc[2 * k] = fmaf(pp[t], lo16(Vb[d][t][k]), acc[2 * k]);
          acc[2 * k + 1] = fmaf(pp[t], hi16(Vb[d][t][k]), acc[2 * k + 1]);
        }
      asm volatile("" : "+v"(acc[0]), "+v"(acc[1]), "+v"(acc[2]), "+v"(acc[3]), "+v"(acc[4]), "+v"(acc[5]), "+v"(acc[6]), "+v"(acc[7]));
      asm volatile("s_waitcnt lgkmcnt(0)" : "+v"(tk));
      const int pg = __builtin_amdgcn_readfirstlane(tk);
      live[d] = pg < NPG;
      tk = take_ticket(ctr_addr);
      const int off = live[d] ? pg * 256 : -(r * 16 + c);
      const int st = live[d] ? 64 : 0;
      __builtin_amdgcn_sched_barrier(0);
#pragma unroll
      for (int t = 0; t < 4; ++t) {
        Kb[d][t] = __builtin_nontemporal_load(kp + off + t * st);
        Vb[d][t] = __builtin_nontemporal_load(vp + off + t * st);
      }
      __builtin_amdgcn_sched_barrier(0);
    }
  } while (live[0]);
  STAMP(0, 2);
  {
    float* rec = red + (wave * 4 + r) * 132;
#pragma unroll
    for (int k = 0; k < 8; ++k) rec[4 + 8 * c + k] = acc[k];
    if (c == 0) { rec[0] = m; rec[1] = l; }
  }
  asm volatile("s_waitcnt vmcnt(0)" ::: "memory");
  __syncthreads();
  const int part = (b * NH + h) * NSPLIT + sp;
  if (tid < DH) {
    float M = -INFINITY;
#pragma unroll
    for (int g = 0; g < 32; ++g) M = fmaxf(M, red[g * 132]);
    float L = 0.f, o = 0.f;
#pragma unroll
    for (int g = 0; g < 32; ++g) {
      const float wgt = __builtin_amdgcn_exp2f(red[g * 132] - M);
      L = fmaf(wgt, red[g * 132 + 1], L);
      o = fmaf(wgt, red[g * 132 + 4 + tid], o);
    }
    os[tid] = o;
    if (tid == 0) { ((float2*)(ws + WS_PML))[part] = make_float2(M, L); }
  }
  __syncthreads();
  {
    float a[8];
#pragma unroll
    for (int k = 0; k < 8; ++k) a[k] = 0.f;
    const u32x4* wl = (const u32x4*)(smem + L_WO) + wave * 16 * 64 + lane;
#pragma unroll
    for (int i = 0; i < 16; ++i) {
      const u32x4 ww = wl[i * 64];
      const float ov = os[wave * 16 + i];
#pragma unroll
      for (int k = 0; k < 4; ++k) { a[2 * k] = fmaf(ov, lo16(ww[k]), a[2 * k]); a[2 * k + 1] = fmaf(ov, hi16(ww[k]), a[2 * k + 1]); }
    }
    *(f32x4*)(red + wave * 512 + lane * 8) = f32x4{a[0], a[1], a[2], a[3]};
    *(f32x4*)(red + wave * 512 + lane * 8 + 4) = f32x4{a[4], a[5], a[6], a[7]};
  }
  __syncthreads();
  {
    float s = 0.f;
#pragma unroll
    for (int g = 0; g < 8; ++g) s += red[g * 512 + tid];
    ((float*)(ws + WS_Y))[((size_t)(tid >> 3) * NPART + part) * 8 + (tid & 7)] = s;
  }
  STAMP(0, 3);
}

#define RLX_AGENT __ATOMIC_RELAXED, __HIP_MEMORY_SCOPE_AGENT
constexpr unsigned SPIN_CAP = 1u << 22;

__global__ void __launch_bounds__(256) k_tail(const unsigned short* __restrict__ x, unsigned char* ws, unsigned short* __restrict__ out) {
  __shared__ __attribute__((aligned(16))) float hs[NB * DM];
  __shared__ __attribute__((aligned(16))) float red[4 * 64];
  __shared__ __attribute__((aligned(16))) float hmine[16];
  const int tid = threadIdx.x, lane = tid & 63, wave = tid >> 6, j = blockIdx.x;
  const int ch = tid & 1, rg = tid >> 1;
  u64* g1 = (u64*)(ws + WS_G1);
  u64* g2 = (u64*)(ws + WS_G2);
  STAMP(1, 0);
  const int jc = j & 63;
  const float2 ml = ((const float2*)(ws + WS_PML))[tid];
  const f32x4* yp = (const f32x4*)(ws + WS_Y) + ((size_t)jc * NPART + tid) * 2;
  const f32x4 y0 = yp[0], y1 = yp[1];
  const unsigned short x0r = x[jc * 8 + (tid & 7)], x1r = x[DM + jc * 8 + (tid & 7)];
  __builtin_amdgcn_sched_barrier(0);
  u32x4 g[4], u[4], d[7];
  {
    const u32x4* tp = (const u32x4*)(ws + WS_GU + (size_t)j * 32768) + tid;
#pragma unroll
    for (int i = 0; i < 4; ++i) { g[i] = tp[i * 512]; u[i] = tp[i * 512 + 256]; }
    const u32x4* dp = (const u32x4*)(ws + WS_DT + (size_t)jc * 28672) + tid;
#pragma unroll
    for (int i = 0; i < 7; ++i) d[i] = dp[i * 256];
  }
  __builtin_amdgcn_sched_barrier(0);
  {
    float M = fmaxf(ml.x, dpp<XOR1>(ml.x)); M = fmaxf(M, dpp<XOR2>(M));
    const float wgt = __builtin_amdgcn_exp2f(ml.x - M);
    float L = wgt * ml.y; L += dpp<XOR1>(L); L += dpp<XOR2>(L);
    const float cf = wgt / L;
    float v[8] = {y0[0] * cf, y0[1] * cf, y0[2] * cf, y0[3] * cf, y1[0] * cf, y1[1] * cf, y1[2] * cf, y1[3] * cf};
#pragma unroll
    for (int k = 0; k < 8; ++k) v[k] = wave_sum(v[k]);
    if (lane == 0) {
#pragma unroll
      for (int k = 0; k < 8; ++k) red[wave * 8 + k] = v[k];
    }
    __syncthreads();
    if (tid < 8 && j < 64) {
      const float s0 = red[tid] + red[8 + tid], s1 = red[16 + tid] + red[24 + tid];
      const unsigned short h0 = f2bf(bf2f(x0r) + rbf(s0)), h1 = f2bf(bf2f(x1r) + rbf(s1));
      hmine[tid] = bf2f(h0); hmine[8 + tid] = bf2f(h1);
      __hip_atomic_store(g1 + j * 8 + tid, (1ull << 32) | ((unsigned)h1 << 16) | (unsigned)h0, RLX_AGENT);
    }
  }
  STAMP(1, 1);
  {
    u64 a0, a1; unsigned spins = 0;
    do {
      a0 = __hip_atomic_load(g1 + tid, RLX_AGENT);
      a1 = __hip_atomic_load(g1 + 256 + tid, RLX_AGENT);
    } while ((((a0 & a1) >> 32) != 1ull) && ++spins < SPIN_CAP);
    hs[tid] = lo16((unsigned)a0); hs[DM + tid] = hi16((unsigned)a0);
    hs[256 + tid] = lo16((unsigned)a1); hs[DM + 256 + tid] = hi16((unsigned)a1);
  }
  __syncthreads();
  STAMP(1, 2);
  float mb0, mb1, den0, den1;
  {
    float v0[8], v1[8], s0 = 0.f, s1 = 0.f;
#pragma unroll
    for (int i = 0; i < 8; ++i) { v0[i] = hs[lane + 64 * i]; v1[i] = hs[DM + lane + 64 * i]; s0 += v0[i]; s1 += v1[i]; }
    s0 = wave_sum(s0); s1 = wave_sum(s1);
    const float mean0 = s0 * (1.0f / 512.0f), mean1 = s1 * (1.0f / 512.0f);
    float q0 = 0.f, q1 = 0.f;
#pragma unroll
    for (int i = 0; i < 8; ++i) { const float d0 = v0[i] - mean0, d1 = v1[i] - mean1; q0 += d0 * d0; q1 += d1 * d1; }
    q0 = wave_sum(q0); q1 = wave_sum(q1);
    mb0 = rbf(mean0); mb1 = rbf(mean1);
    den0 = rbf(sqrtf(rbf(rbf(q0 * (1.0f / 512.0f)) + 1e-5f)));
    den1 = rbf(sqrtf(rbf(rbf(q1 * (1.0f / 512.0f)) + 1e-5f)));
  }
  {
    float a[4][8];
#pragma unroll
    for (int qq = 0; qq < 4; ++qq)
#pragma unroll
      for (int k = 0; k < 8; ++k) a[qq][k] = 0.f;
#pragma unroll
    for (int i = 0; i < 4; ++i) {
      const float x0 = rbf(rbf(hs[rg + 128 * i] - mb0) / den0), x1 = rbf(rbf(hs[DM + rg + 128 * i] - mb1) / den1);
#pragma unroll
      for (int k = 0; k < 4; ++k) {
        a[0][2 * k] = fmaf(x0, lo16(g[i][k]), a[0][2 * k]); a[0][2 * k + 1] = fmaf(x0, hi16(g[i][k]), a[0][2 * k + 1]);
        a[1][2 * k] = fmaf(x0, lo16(u[i][k]), a[1][2 * k]); a[1][2 * k + 1] = fmaf(x0, hi16(u[i][k]), a[1][2 * k + 1]);
        a[2][2 * k] = fmaf(x1, lo16(g[i][k]), a[2][2 * k]); a[2][2 * k + 1] = fmaf(x1, hi16(g[i][k]), a[2][2 * k + 1]);
        a[3][2 * k] = fmaf(x1, lo16(u[i][k]), a[3][2 * k]); a[3][2 * k + 1] = fmaf(x1, hi16(u[i][k]), a[3][2 * k + 1]);
      }
    }
#pragma unroll
    for (int qq = 0; qq < 4; ++qq)
#pragma unroll
      for (int k = 0; k < 8; ++k) {
        float v = a[qq][k];
        v += dpp<XOR2>(v); v += dpp<0x124>(v)  ; v += dpp<XOR8>(v);
        a[qq][k] = xrow_sum(v);
      }
    __syncthreads();
    if (lane < 2) {
#pragma unroll
      for (int qq = 0; qq < 4; ++qq)
#pragma unroll
        for (int k = 0; k < 8; ++k) red[wave * 64 + qq * 16 + lane * 8 + k] = a[qq][k];
    }
    __syncthreads();
    if (tid < 16) {
      float g0 = 0.f, u0 = 0.f, g1v = 0.f, u1 = 0.f;
#pragma unroll
      for (int w2 = 0; w2 < 4; ++w2) { g0 += red[w2 * 64 + tid]; u0 += red[w2 * 64 + 16 + tid]; g1v += red[w2 * 64 + 32 + tid]; u1 += red[w2 * 64 + 48 + tid]; }
      g0 = rbf(g0); u0 = rbf(u0); g1v = rbf(g1v); u1 = rbf(u1);
      const unsigned short a0 = f2bf(rbf(g0 / (1.0f + __expf(-g0))) * u0), a1 = f2bf(rbf(g1v / (1.0f + __expf(-g1v))) * u1);
      __hip_atomic_store(g2 + j * 16 + tid, (1ull << 32) | ((unsigned)a1 << 16) | (unsigned)a0, RLX_AGENT);
    }
  }
  STAMP(1, 3);
  if (j >= 64) return;
  {
    u64 av[7]; unsigned spins = 0; bool ok;
    do {
      ok = true;
#pragma unroll
      for (int i = 0; i < 7; ++i) { av[i] = __hip_atomic_load(g2 + tid + 256 * i, RLX_AGENT); }
#pragma unroll
      for (int i = 0; i < 7; ++i) ok &= (av[i] >> 32) == 1ull;
    } while (!ok && ++spins < SPIN_CAP);
    STAMP(1, 4);
    float a[2][8];
#pragma unroll
    for (int k = 0; k < 8; ++k) { a[0][k] = 0.f; a[1][k] = 0.f; }
#pragma unroll
    for (int i = 0; i < 7; ++i) {
      const float a0 = lo16((unsigned)av[i]), a1 = hi16((unsigned)av[i]);
#pragma unroll
      for (int k = 0; k < 4; ++k) {
        a[0][2 * k] = fmaf(a0, lo16(d[i][k]), a[0][2 * k]); a[0][2 * k + 1] = fmaf(a0, hi16(d[i][k]), a[0][2 * k + 1]);
        a[1][2 * k] = fmaf(a1, lo16(d[i][k]), a[1][2 * k]); a[1][2 * k + 1] = fmaf(a1, hi16(d[i][k]), a[1][2 * k + 1]);
      }
    }
#pragma unroll
    for (int qq = 0; qq < 2; ++qq)
#pragma unroll
      for (int k = 0; k < 8; ++k) a[qq][k] = wave_sum(a[qq][k]);
    __syncthreads();
    if (lane == 0) {
#pragma unroll
      for (int qq = 0; qq < 2; ++qq)
#pragma unroll
        for (int k = 0; k < 8; ++k) red[wave * 16 + qq * 8 + k] = a[qq][k];
    }
    __syncthreads();
    if (tid < 16) {
      const float s = (red[tid] + red[16 + tid]) + (red[32 + tid] + red[48 + tid]);
      out[(tid >> 3) * DM + j * 8 + (tid & 7)] = f2bf(hmine[tid] + rbf(s));
    }
    STAMP(1, 5);
  }
}

extern "C" void kernel_launch(void* const* d_in, const int* in_sizes, int n_in,
                              void* d_out, int out_size, void* d_ws, size_t ws_size,
                              hipStream_t stream) {
  const unsigned short* x = (const unsigned short*)d_in[0];
  const unsigned short* kc = (const unsigned short*)d_in[1];
  const unsigned short* vc = (const unsigned short*)d_in[2];
  const unsigned short* wq = (const unsigned short*)d_in[3];
  const unsigned short* wo = (const unsigned short*)d_in[4];
  const unsigned short* wg = (const unsigned short*)d_in[6];
  const unsigned short* wu = (const unsigned short*)d_in[7];
  const unsigned short* wd = (const unsigned short*)d_in[8];
  unsigned char* ws = (unsigned char*)d_ws;
  k_attn<<<256, NTHR, 0, stream>>>(x, kc, vc, wq, wo, wg, wu, wd, ws);
  k_tail<<<112, 256, 0, stream>>>(x, ws, (unsigned short*)d_out);
}
```

```cpp
#include <hip/hip_runtime.h>
#include <hip/hip_bf16.h>
#include <stdint.h>

typedef unsigned u32x4 __attribute__((ext_vector_type(4)));
typedef float f32x4 __attribute__((ext_vector_type(4)));
typedef __bf16 bf16x2 __attribute__((ext_vector_type(2)));
typedef unsigned long long u64;

constexpr int NB = 2, DM = 512, NH = 32, SK = 16384, DH = 128, FF = 1792;
constexpr int NSPLIT = 4;
constexpr int KPS = SK / NSPLIT;
constexpr int NWAVE = 8, NTHR = 512;
constexpr int PAGE = 16;
constexpr int NPAGE = KPS / NWAVE / PAGE;
constexpr int NPART = NB * NH * NSPLIT;

__device__ __forceinline__ float bf2f(unsigned short b) { return __builtin_bit_cast(float, (unsigned)b << 16); }
__device__ __forceinline__ float rbf(float x) { return __bfloat162float(__float2bfloat16(x)); }
__device__ __forceinline__ unsigned short f2bf(float x) { return __builtin_bit_cast(unsigned short, __float2bfloat16(x)); }
__device__ __forceinline__ float lo16(unsigned u) { return __builtin_bit_cast(float, u << 16); }
__device__ __forceinline__ float hi16(unsigned u) { return __builtin_bit_cast(float, u & 0xffff0000u); }
__device__ __forceinline__ float dot2(unsigned k, unsigned q, float acc) {
  return __builtin_amdgcn_fdot2_f32_bf16(__builtin_bit_cast(bf16x2, k), __builtin_bit_cast(bf16x2, q), acc, false);
}
template <int CTRL> __device__ __forceinline__ float dpp(float x) {
  return __builtin_bit_cast(float, __builtin_amdgcn_mov_dpp(__builtin_bit_cast(int, x), CTRL, 0xf, 0xf, true));
}
constexpr int XOR1 = 0xB1, XOR2 = 0x4E, XOR7 = 0x141, XOR8 = 0x128;
__device__ __forceinline__ float row16_sum(float x) {
  x += dpp<XOR1>(x); x += dpp<XOR2>(x); x += dpp<XOR7>(x); x += dpp<XOR8>(x); return x;
}
__device__ __forceinline__ float xrow_sum(float x) {
  auto s = __builtin_amdgcn_permlane16_swap(__float_as_uint(x), __float_as_uint(x), false, false);
  x = __uint_as_float(s[0]) + __uint_as_float(s[1]);
  auto t = __builtin_amdgcn_permlane32_swap(__float_as_uint(x), __float_as_uint(x), false, false);
  return __uint_as_float(t[0]) + __uint_as_float(t[1]);
}
__device__ __forceinline__ float wave_sum(float x) { return xrow_sum(row16_sum(x)); }

#define STAMP(k, i)
#define LOOP_BARRIER 1
#define ABL_NOWO 0
#define ABL_NORT 0
#define ABL_NOQ 0
#define ABL_NOCOMP 0
constexpr size_t WS_G1 = 0;
constexpr size_t WS_G2 = 4096;
constexpr size_t WS_GEND = WS_G2 + FF * 8;
constexpr size_t WS_PML = 32768;
constexpr size_t WS_Y = 65536;
constexpr size_t WS_GU = 1u << 20;
constexpr size_t WS_DT = 5u << 20;

constexpr int L_WO = 0, L_RED = 131072, L_XS = L_RED + 32 * 132 * 4, L_LN = L_XS + 2048, L_Q = L_LN + 2048, L_O = L_Q + 256, L_CTR = L_O + 512, L_END = L_CTR + 16;

__device__ __forceinline__ void glds16(const void* gsrc, unsigned lds_dst) {
  unsigned keep;
  asm volatile("s_mov_b32 %0, m0\n\ts_mov_b32 m0, %2\n\ts_nop 0\n\tglobal_load_lds_dwordx4 %1, off\n\ts_mov_b32 m0, %0" : "=&s"(keep) : "v"(gsrc), "s"(lds_dst) : "memory");
}

__device__ __forceinline__ int take_ticket(unsigned lds_addr) {
  int tk; unsigned long long save;
  asm volatile("s_mov_b64 %1, exec\n\ts_mov_b64 exec, 1\n\tds_add_rtn_u32 %0, %2, %3\n\ts_mov_b64 exec, %1"
               : "=&v"(tk), "=&s"(save) : "v"(lds_addr), "v"(1u) : "memory");
  return tk;
}

__global__ void __launch_bounds__(NTHR, 2) k_attn(const unsigned short* __restrict__ x, const unsigned short* __restrict__ kc,
                                                  const unsigned short* __restrict__ vc, const unsigned short* __restrict__ wq,
                                                  const unsigned short* __restrict__ wo, const unsigned short* __restrict__ wg,
                                                  const unsigned short* __restrict__ wu, const unsigned short* __restrict__ wd,
                                                  unsigned char* __restrict__ ws) {
  __shared__ __attribute__((aligned(16))) unsigned char smem[L_END];
  float* red = (float*)(smem + L_RED);
  float* os = (float*)(smem + L_O);
  const int tid = threadIdx.x, lane = tid & 63, wave = __builtin_amdgcn_readfirstlane(tid >> 6), r = lane >> 4, c = lane & 15;
  const int bid = blockIdx.x;
  const int b = bid >> 7, h = (bid >> 2) & 31, sp = bid & 3;
  STAMP(0, 0);
  const u32x4 xr = *(const u32x4*)(x + b * DM + lane * 8);
  const int rg = tid >> 4;
  u32x4 w[16];
  {
    const u32x4* wp = (const u32x4*)(wq + (size_t)h * DH) + c;
#pragma unroll
    for (int i = 0; i < 16; ++i) w[i] = wp[ABL_NOQ ? 0 : (size_t)(rg + 32 * i) * (NH * DH / 8)];
  }
  __builtin_amdgcn_sched_barrier(0);
  const size_t base = ((size_t)(b * NH + h) * SK + (size_t)sp * KPS) * DH;
  const u32x4* kp = (const u32x4*)(kc + base) + wave * 256 + r * 16 + c;
  const u32x4* vp = (const u32x4*)(vc + base) + wave * 256 + r * 16 + c;
  constexpr int DEPTH = 4;
  u32x4 Kb[DEPTH][4], Vb[DEPTH][4];
#pragma unroll
  for (int t = 0; t < 4; ++t) {
    Kb[0][t] = __builtin_nontemporal_load(kp + t * 64);
    Vb[0][t] = __builtin_nontemporal_load(vp + t * 64);
  }
  __builtin_amdgcn_sched_barrier(0);
  float* lnw = (float*)(smem + L_WO) + wave * 512;
  {
    float v[8];
#pragma unroll
    for (int k = 0; k < 4; ++k) { v[2 * k] = lo16(xr[k]); v[2 * k + 1] = hi16(xr[k]); }
    float s = ((v[0] + v[1]) + (v[2] + v[3])) + ((v[4] + v[5]) + (v[6] + v[7]));
    s = wave_sum(s);
    const float mean = s * (1.0f / 512.0f);
    float ss = 0.f;
#pragma unroll
    for (int i = 0; i < 8; ++i) { const float d = v[i] - mean; ss += d * d; }
    ss = wave_sum(ss);
    const float var = rbf(ss * (1.0f / 512.0f)), mb = rbf(mean);
    const float den = rbf(sqrtf(rbf(var + 1e-5f)));
#pragma unroll
    for (int i = 0; i < 8; ++i) v[i] = rbf(rbf(v[i] - mb) / den);
    *(f32x4*)(lnw + lane * 8) = f32x4{v[0], v[1], v[2], v[3]};
    *(f32x4*)(lnw + lane * 8 + 4) = f32x4{v[4], v[5], v[6], v[7]};
  }
  asm volatile("s_waitcnt lgkmcnt(0)" ::: "memory");
  STAMP(0, 4);
  {
    float a[8];
#pragma unroll
    for (int k = 0; k < 8; ++k) a[k] = 0.f;
#pragma unroll
    for (int i = 0; i < 16; ++i) {
      const float xl = lnw[rg + 32 * i];
#pragma unroll
      for (int k = 0; k < 4; ++k) { a[2 * k] = fmaf(xl, lo16(w[i][k]), a[2 * k]); a[2 * k + 1] = fmaf(xl, hi16(w[i][k]), a[2 * k + 1]); }
    }
#pragma unroll
    for (int k = 0; k < 8; ++k) a[k] = xrow_sum(a[k]);
    if (r == 0) {
      *(f32x4*)(red + wave * 128 + c * 8) = f32x4{a[0], a[1], a[2], a[3]};
      *(f32x4*)(red + wave * 128 + c * 8 + 4) = f32x4{a[4], a[5], a[6], a[7]};
    }
  }
  STAMP(0, 6);
  __syncthreads();
  STAMP(0, 7);
  u32x4 q;
  {
    f32x4 s0 = {0, 0, 0, 0}, s1 = {0, 0, 0, 0};
#pragma unroll
    for (int g = 0; g < 8; ++g) { s0 += *(const f32x4*)(red + g * 128 + c * 8); s1 += *(const f32x4*)(red + g * 128 + c * 8 + 4); }
    q[0] = (unsigned)f2bf(s0[0]) | ((unsigned)f2bf(s0[1]) << 16); q[1] = (unsigned)f2bf(s0[2]) | ((unsigned)f2bf(s0[3]) << 16);
    q[2] = (unsigned)f2bf(s1[0]) | ((unsigned)f2bf(s1[1]) << 16); q[3] = (unsigned)f2bf(s1[2]) | ((unsigned)f2bf(s1[3]) << 16);
  }
  STAMP(0, 1);
  u32x4 tl[4];
  const bool job_gu = !ABL_NORT && bid < 128, job_d = !ABL_NORT && bid >= 128 && bid < 184;
#pragma unroll
  for (int i = 0; i < 4; ++i) {
    const int u = tid + 512 * i;
    const unsigned short* s = x;
    if (job_gu) {
      const int uu = u < 1792 ? u : u - 256;
      const int mat = uu / 896, rem = uu - mat * 896;
      s = (mat ? wu : wg) + (size_t)bid * 4 * FF + rem * 8;
    } else if (job_d) {
      s = wd + (size_t)(bid - 128) * 32 * DM + u * 8;
    }
    tl[i] = *(const u32x4*)s;
  }
  __builtin_amdgcn_sched_barrier(0);
#pragma unroll
  for (int d = 1; d < DEPTH; ++d)
#pragma unroll
    for (int t = 0; t < 4; ++t) {
      Kb[d][t] = __builtin_nontemporal_load(kp + d * 2048 + t * 64);
      Vb[d][t] = __builtin_nontemporal_load(vp + d * 2048 + t * 64);
    }
  __builtin_amdgcn_sched_barrier(0);
  u32x4* stg = (u32x4*)(smem + L_WO + 32768);
#pragma unroll
  for (int i = 0; i < 4; ++i) { const int u = tid + 512 * i; stg[u + (u >> 5)] = tl[i]; }
  if (bid == 255) {
    for (int i = tid; i < (int)(WS_GEND / 16); i += NTHR) ((u32x4*)ws)[i] = u32x4{0u, 0u, 0u, 0u};
  }
  __builtin_amdgcn_sched_barrier(0);
  __syncthreads();
  if (job_gu) {
#pragma unroll
    for (int i = 0; i < 4; ++i) {
      const int v = tid + 512 * i;
      if (v < 1792) {
        const int o = v >> 3, e = v & 7, jt = o >> 1, mat = o & 1, rr = e >> 1, ch = e & 1;
        const int u = mat * 896 + rr * 224 + 2 * jt + ch, row = bid * 4 + rr;
        *(u32x4*)(ws + WS_GU + (size_t)jt * 32768 + (((row >> 7) * 2 + mat) * 256 + 2 * (row & 127) + ch) * 16) = stg[u + (u >> 5)];
      }
    }
  } else if (job_d) {
#pragma unroll
    for (int i = 0; i < 4; ++i) {
      const int v = tid + 512 * i, cu = v >> 5, rr = v & 31, u = rr * 64 + cu;
      *(u32x4*)(ws + WS_DT + (size_t)cu * 28672 + ((bid - 128) * 32 + rr) * 16) = stg[u + (u >> 5)];
    }
  }
  if (job_gu || job_d) __syncthreads();
  {
    const unsigned char* src = (const unsigned char*)(wo + ((size_t)h * DH + wave * 16) * DM) + lane * 16;
    const unsigned dst = (unsigned)(uintptr_t)((__attribute__((address_space(3))) unsigned char*)(smem + L_WO)) + wave * 16 * 1024;
#pragma unroll
    for (int i = 0; i < 16; ++i) { if (!ABL_NOWO) glds16(src + i * 1024, dst + i * 1024); }
  }
  constexpr float SCALE = 0.08838834764831845f, L2E = 1.4426950408889634f;
  float m = -INFINITY, l = 0.f, acc[8];
#pragma unroll
  for (int k = 0; k < 8; ++k) acc[k] = 0.f;
  constexpr int NIT = NPAGE / DEPTH;
  for (int it = 0; it < NIT; ++it) {
    const bool more = it + 1 < NIT;
    const int off = more ? (it + 1) * DEPTH * 2048 : -(wave * 256 + r * 16 + c);
    const int sd = more ? 2048 : 0, st = more ? 64 : 0;
#pragma unroll
    for (int d = 0; d < DEPTH; ++d) {
      float s[4];
#pragma unroll
      for (int t = 0; t < 4; ++t) {
        float dd = 0.f;
#pragma unroll
        for (int k = 0; k < 4; ++k) dd = dot2(Kb[d][t][k], q[k], dd);
        dd = row16_sum(dd);
        s[t] = rbf(rbf(dd) * SCALE) * L2E;
      }
      const float pm = fmaxf(fmaxf(s[0], s[1]), fmaxf(s[2], s[3]));
      const float mn = fmaxf(m, pm);
      const float alpha = __builtin_amdgcn_exp2f(m - mn);
      m = mn;
      float pp[4];
#pragma unroll
      for (int t = 0; t < 4; ++t) pp[t] = __builtin_amdgcn_exp2f(s[t] - mn);
      l = fmaf(l, alpha, (pp[0] + pp[1]) + (pp[2] + pp[3]));
#pragma unroll
      for (int k = 0; k < 8; ++k) acc[k] *= alpha;
#pragma unroll
      for (int t = 0; t < 4; ++t)
#pragma unroll
        for (int k = 0; k < 4; ++k) {
          acc[2 * k] = fmaf(pp[t], lo16(Vb[d][t][k]), acc[2 * k]);
          acc[2 * k + 1] = fmaf(pp[t], hi16(Vb[d][t][k]), acc[2 * k + 1]);
        }
      asm volatile("" : "+v"(acc[0]), "+v"(acc[1]), "+v"(acc[2]), "+v"(acc[3]), "+v"(acc[4]), "+v"(acc[5]), "+v"(acc[6]), "+v"(acc[7]));
      __builtin_amdgcn_sched_barrier(0);
#pragma unroll
      for (int t = 0; t < 4; ++t) {
        Kb[d][t] = __builtin_nontemporal_load(kp + off + d * sd + t * st);
        Vb[d][t] = __builtin_nontemporal_load(vp + off + d * sd + t * st);
      }
      __builtin_amdgcn_sched_barrier(0);
    }
    __builtin_amdgcn_s_barrier();
  }
  STAMP(0, 2);
  {
    float* rec = red + (wave * 4 + r) * 132;
#pragma unroll
    for (int k = 0; k < 8; ++k) rec[4 + 8 * c + k] = acc[k];
    if (c == 0) { rec[0] = m; rec[1] = l; }
  }
  asm volatile("s_waitcnt vmcnt(0)" ::: "memory");
  __syncthreads();
  const int part = (b * NH + h) * NSPLIT + sp;
  if (tid < DH) {
    float M = -INFINITY;
#pragma unroll
    for (int g = 0; g < 32; ++g) M = fmaxf(M, red[g * 132]);
    float L = 0.f, o = 0.f;
#pragma unroll
    for (int g = 0; g < 32; ++g) {
      const float wgt = __builtin_amdgcn_exp2f(red[g * 132] - M);
      L = fmaf(wgt, red[g * 132 + 1], L);
      o = fmaf(wgt, red[g * 132 + 4 + tid], o);
    }
    os[tid] = o;
    if (tid == 0) { ((float2*)(ws + WS_PML))[part] = make_float2(M, L); }
  }
  __syncthreads();
  {
    float a[8];
#pragma unroll
    for (int k = 0; k < 8; ++k) a[k] = 0.f;
    const u32x4* wl = (const u32x4*)(smem + L_WO) + wave * 16 * 64 + lane;
#pragma unroll
    for (int i = 0; i < 16; ++i) {
      const u32x4 ww = wl[i * 64];
      const float ov = os[wave * 16 + i];
#pragma unroll
      for (int k = 0; k < 4; ++k) { a[2 * k] = fmaf(ov, lo16(ww[k]), a[2 * k]); a[2 * k + 1] = fmaf(ov, hi16(ww[k]), a[2 * k + 1]); }
    }
    *(f32x4*)(red + wave * 512 + lane * 8) = f32x4{a[0], a[1], a[2], a[3]};
    *(f32x4*)(red + wave * 512 + lane * 8 + 4) = f32x4{a[4], a[5], a[6], a[7]};
  }
  __syncthreads();
  {
    float s = 0.f;
#pragma unroll
    for (int g = 0; g < 8; ++g) s += red[g * 512 + tid];
    ((float*)(ws + WS_Y))[((size_t)(tid >> 3) * NPART + part) * 8 + (tid & 7)] = s;
  }
  STAMP(0, 3);
}

#define RLX_AGENT __ATOMIC_RELAXED, __HIP_MEMORY_SCOPE_AGENT
constexpr unsigned SPIN_CAP = 1u << 22;

__global__ void __launch_bounds__(256) k_tail(const unsigned short* __restrict__ x, unsigned char* ws, unsigned short* __restrict__ out) {
  __shared__ __attribute__((aligned(16))) float hs[NB * DM];
  __shared__ __attribute__((aligned(16))) float red[4 * 64];
  __shared__ __attribute__((aligned(16))) float hmine[16];
  const int tid = threadIdx.x, lane = tid & 63, wave = tid >> 6, j = blockIdx.x;
  const int ch = tid & 1, rg = tid >> 1;
  u64* g1 = (u64*)(ws + WS_G1);
  u64* g2 = (u64*)(ws + WS_G2);
  STAMP(1, 0);
  const int jc = j & 63;
  const float2 ml = ((const float2*)(ws + WS_PML))[tid];
  const f32x4* yp = (const f32x4*)(ws + WS_Y) + ((size_t)jc * NPART + tid) * 2;
  const f32x4 y0 = yp[0], y1 = yp[1];
  const unsigned short x0r = x[jc * 8 + (tid & 7)], x1r = x[DM + jc * 8 + (tid & 7)];
  __builtin_amdgcn_sched_barrier(0);
  u32x4 g[4], u[4], d[7];
  {
    const u32x4* tp = (const u32x4*)(ws + WS_GU + (size_t)j * 32768) + tid;
#pragma unroll
    for (int i = 0; i < 4; ++i) { g[i] = tp[i * 512]; u[i] = tp[i * 512 + 256]; }
    const u32x4* dp = (const u32x4*)(ws + WS_DT + (size_t)jc * 28672) + tid;
#pragma unroll
    for (int i = 0; i < 7; ++i) d[i] = dp[i * 256];
  }
  __builtin_amdgcn_sched_barrier(0);
  {
    float M = fmaxf(ml.x, dpp<XOR1>(ml.x)); M = fmaxf(M, dpp<XOR2>(M));
    const float wgt = __builtin_amdgcn_exp2f(ml.x - M);
    float L = wgt * ml.y; L += dpp<XOR1>(L); L += dpp<XOR2>(L);
    const float cf = wgt / L;
    float v[8] = {y0[0] * cf, y0[1] * cf, y0[2] * cf, y0[3] * cf, y1[0] * cf, y1[1] * cf, y1[2] * cf, y1[3] * cf};
#pragma unroll
    for (int k = 0; k < 8; ++k) v[k] = wave_sum(v[k]);
    if (lane == 0) {
#pragma unroll
      for (int k = 0; k < 8; ++k) red[wave * 8 + k] = v[k];
    }
    __syncthreads();
    if (tid < 8 && j < 64) {
      const float s0 = red[tid] + red[8 + tid], s1 = red[16 + tid] + red[24 + tid];
      const unsigned short h0 = f2bf(bf2f(x0r) + rbf(s0)), h1 = f2bf(bf2f(x1r) + rbf(s1));
      hmine[tid] = bf2f(h0); hmine[8 + tid] = bf2f(h1);
      __hip_atomic_store(g1 + j * 8 + tid, (1ull << 32) | ((unsigned)h1 << 16) | (unsigned)h0, RLX_AGENT);
    }
  }
  STAMP(1, 1);
  {
    u64 a0, a1; unsigned spins = 0;
    do {
      a0 = __hip_atomic_load(g1 + tid, RLX_AGENT);
      a1 = __hip_atomic_load(g1 + 256 + tid, RLX_AGENT);
    } while ((((a0 & a1) >> 32) != 1ull) && ++spins < SPIN_CAP);
    hs[tid] = lo16((unsigned)a0); hs[DM + tid] = hi16((unsigned)a0);
    hs[256 + tid] = lo16((unsigned)a1); hs[DM + 256 + tid] = hi16((unsigned)a1);
  }
  __syncthreads();
  STAMP(1, 2);
  float mb0, mb1, den0, den1;
  {
    float v0[8], v1[8], s0 = 0.f, s1 = 0.f;
#pragma unroll
    for (int i = 0; i < 8; ++i) { v0[i] = hs[lane + 64 * i]; v1[i] = hs[DM + lane + 64 * i]; s0 += v0[i]; s1 += v1[i]; }
    s0 = wave_sum(s0); s1 = wave_sum(s1);
    const float mean0 = s0 * (1.0f / 512.0f), mean1 = s1 * (1.0f / 512.0f);
    float q0 = 0.f, q1 = 0.f;
#pragma unroll
    for (int i = 0; i < 8; ++i) { const float d0 = v0[i] - mean0, d1 = v1[i] - mean1; q0 += d0 * d0; q1 += d1 * d1; }
    q0 = wave_sum(q0); q1 = wave_sum(q1);
    mb0 = rbf(mean0); mb1 = rbf(mean1);
    den0 = rbf(sqrtf(rbf(rbf(q0 * (1.0f / 512.0f)) + 1e-5f)));
    den1 = rbf(sqrtf(rbf(rbf(q1 * (1.0f / 512.0f)) + 1e-5f)));
  }
  {
    float a[4][8];
#pragma unroll
    for (int qq = 0; qq < 4; ++qq)
#pragma unroll
      for (int k = 0; k < 8; ++k) a[qq][k] = 0.f;
#pragma unroll
    for (int i = 0; i < 4; ++i) {
      const float x0 = rbf(rbf(hs[rg + 128 * i] - mb0) / den0), x1 = rbf(rbf(hs[DM + rg + 128 * i] - mb1) / den1);
#pragma unroll
      for (int k = 0; k < 4; ++k) {
        a[0][2 * k] = fmaf(x0, lo16(g[i][k]), a[0][2 * k]); a[0][2 * k + 1] = fmaf(x0, hi16(g[i][k]), a[0][2 * k + 1]);
        a[1][2 * k] = fmaf(x0, lo16(u[i][k]), a[1][2 * k]); a[1][2 * k + 1] = fmaf(x0, hi16(u[i][k]), a[1][2 * k + 1]);
        a[2][2 * k] = fmaf(x1, lo16(g[i][k]), a[2][2 * k]); a[2][2 * k + 1] = fmaf(x1, hi16(g[i][k]), a[2][2 * k + 1]);
        a[3][2 * k] = fmaf(x1, lo16(u[i][k]), a[3][2 * k]); a[3][2 * k + 1] = fmaf(x1, hi16(u[i][k]), a[3][2 * k + 1]);
      }
    }
#pragma unroll
    for (int qq = 0; qq < 4; ++qq)
#pragma unroll
      for (int k = 0; k < 8; ++k) {
        float v = a[qq][k];
        v += dpp<XOR2>(v); v += dpp<0x124>(v)  ; v += dpp<XOR8>(v);
        a[qq][k] = xrow_sum(v);
      }
    __syncthreads();
    if (lane < 2) {
#pragma unroll
      for (int qq = 0; qq < 4; ++qq)
#pragma unroll
        for (int k = 0; k < 8; ++k) red[wave * 64 + qq * 16 + lane * 8 + k] = a[qq][k];
    }
    __syncthreads();
    if (tid < 16) {
      float g0 = 0.f, u0 = 0.f, g1v = 0.f, u1 = 0.f;
#pragma unroll
      for (int w2 = 0; w2 < 4; ++w2) { g0 += red[w2 * 64 + tid]; u0 += red[w2 * 64 + 16 + tid]; g1v += red[w2 * 64 + 32 + tid]; u1 += red[w2 * 64 + 48 + tid]; }
      g0 = rbf(g0); u0 = rbf(u0); g1v = rbf(g1v); u1 = rbf(u1);
      const unsigned short a0 = f2bf(rbf(g0 / (1.0f + __expf(-g0))) * u0), a1 = f2bf(rbf(g1v / (1.0f + __expf(-g1v))) * u1);
      __hip_atomic_store(g2 + j * 16 + tid, (1ull << 32) | ((unsigned)a1 << 16) | (unsigned)a0, RLX_AGENT);
    }
  }
  STAMP(1, 3);
  if (j >= 64) return;
  {
    u64 av[7]; unsigned spins = 0; bool ok;
    do {
      ok = true;
#pragma unroll
      for (int i = 0; i < 7; ++i) { av[i] = __hip_atomic_load(g2 + tid + 256 * i, RLX_AGENT); }
#pragma unroll
      for (int i = 0; i < 7; ++i) ok &= (av[i] >> 32) == 1ull;
    } while (!ok && ++spins < SPIN_CAP);
    STAMP(1, 4);
    float a[2][8];
#pragma unroll
    for (int k = 0; k < 8; ++k) { a[0][k] = 0.f; a[1][k] = 0.f; }
#pragma unroll
    for (int i = 0; i < 7; ++i) {
      const float a0 = lo16((unsigned)av[i]), a1 = hi16((unsigned)av[i]);
#pragma unroll
      for (int k = 0; k < 4; ++k) {
        a[0][2 * k] = fmaf(a0, lo16(d[i][k]), a[0][2 * k]); a[0][2 * k + 1] = fmaf(a0, hi16(d[i][k]), a[0][2 * k + 1]);
        a[1][2 * k] = fmaf(a1, lo16(d[i][k]), a[1][2 * k]); a[1][2 * k + 1] = fmaf(a1, hi16(d[i][k]), a[1][2 * k + 1]);
      }
    }
#pragma unroll
    for (int qq = 0; qq < 2; ++qq)
#pragma unroll
      for (int k = 0; k < 8; ++k) a[qq][k] = wave_sum(a[qq][k]);
    __syncthreads();
    if (lane == 0) {
#pragma unroll
      for (int qq = 0; qq < 2; ++qq)
#pragma unroll
        for (int k = 0; k < 8; ++k) red[wave * 16 + qq * 8 + k] = a[qq][k];
    }
    __syncthreads();
    if (tid < 16) {
      const float s = (red[tid] + red[16 + tid]) + (red[32 + tid] + red[48 + tid]);
      out[(tid >> 3) * DM + j * 8 + (tid & 7)] = f2bf(hmine[tid] + rbf(s));
    }
    STAMP(1, 5);
  }
}

extern "C" void kernel_launch(void* const* d_in, const int* in_sizes, int n_in,
                              void* d_out, int out_size, void* d_ws, size_t ws_size,
                              hipStream_t stream) {
  const unsigned short* x = (const unsigned short*)d_in[0];
  const unsigned short* kc = (const unsigned short*)d_in[1];
  const unsigned short* vc = (const unsigned short*)d_in[2];
  const unsigned short* wq = (const unsigned short*)d_in[3];
  const unsigned short* wo = (const unsigned short*)d_in[4];
  const unsigned short* wg = (const unsigned short*)d_in[6];
  const unsigned short* wu = (const unsigned short*)d_in[7];
  const unsigned short* wd = (const unsigned short*)d_in[8];
  unsigned char* ws = (unsigned char*)d_ws;
  k_attn<<<256, NTHR, 0, stream>>>(x, kc, vc, wq, wo, wg, wu, wd, ws);
  k_tail<<<112, 256, 0, stream>>>(x, ws, (unsigned short*)d_out);
}
```

```cpp
#include <hip/hip_runtime.h>
#include <hip/hip_bf16.h>
#include <stdint.h>

typedef unsigned u32x4 __attribute__((ext_vector_type(4)));
typedef float f32x4 __attribute__((ext_vector_type(4)));
typedef __bf16 bf16x2 __attribute__((ext_vector_type(2)));
typedef unsigned long long u64;

constexpr int NB = 2, DM = 512, NH = 32, SK = 16384, DH = 128, FF = 1792;
constexpr int NSPLIT = 4;
constexpr int KPS = SK / NSPLIT;
constexpr int NWAVE = 8, NTHR = 512;
constexpr int PAGE = 16;
constexpr int NPAGE = KPS / NWAVE / PAGE;
constexpr int NPART = NB * NH * NSPLIT;

__device__ __forceinline__ float bf2f(unsigned short b) { return __builtin_bit_cast(float, (unsigned)b << 16); }
__device__ __forceinline__ float rbf(float x) { return __bfloat162float(__float2bfloat16(x)); }
__device__ __forceinline__ unsigned short f2bf(float x) { return __builtin_bit_cast(unsigned short, __float2bfloat16(x)); }
__device__ __forceinline__ float lo16(unsigned u) { return __builtin_bit_cast(float, u << 16); }
__device__ __forceinline__ float hi16(unsigned u) { return __builtin_bit_cast(float, u & 0xffff0000u); }
__device__ __forceinline__ float dot2(unsigned k, unsigned q, float acc) {
  return __builtin_amdgcn_fdot2_f32_bf16(__builtin_bit_cast(bf16x2, k), __builtin_bit_cast(bf16x2, q), acc, false);
}
template <int CTRL> __device__ __forceinline__ float dpp(float x) {
  return __builtin_bit_cast(float, __builtin_amdgcn_mov_dpp(__builtin_bit_cast(int, x), CTRL, 0xf, 0xf, true));
}
constexpr int XOR1 = 0xB1, XOR2 = 0x4E, XOR7 = 0x141, XOR8 = 0x128, ROR4 = 0x124;
__device__ __forceinline__ float row16_sum(float x) {
  x += dpp<XOR1>(x); x += dpp<XOR2>(x); x += dpp<XOR7>(x); x += dpp<XOR8>(x); return x;
}
__device__ __forceinline__ float xrow_sum(float x) {
  auto s = __builtin_amdgcn_permlane16_swap(__float_as_uint(x), __float_as_uint(x), false, false);
  x = __uint_as_float(s[0]) + __uint_as_float(s[1]);
  auto t = __builtin_amdgcn_permlane32_swap(__float_as_uint(x), __float_as_uint(x), false, false);
  return __uint_as_float(t[0]) + __uint_as_float(t[1]);
}
__device__ __forceinline__ float xrow_max(float x) {
  auto s = __builtin_amdgcn_permlane16_swap(__float_as_uint(x), __float_as_uint(x), false, false);
  x = fmaxf(__uint_as_float(s[0]), __uint_as_float(s[1]));
  auto t = __builtin_amdgcn_permlane32_swap(__float_as_uint(x), __float_as_uint(x), false, false);
  return fmaxf(__uint_as_float(t[0]), __uint_as_float(t[1]));
}
__device__ __forceinline__ float wave_sum(float x) { return xrow_sum(row16_sum(x)); }

#define STAMP(k, i)
constexpr size_t WS_G0 = 0;
constexpr size_t WS_G1 = 262144;
constexpr size_t WS_G2 = WS_G1 + 4096;
constexpr size_t WS_GEND = WS_G2 + FF * 8;
constexpr size_t WS_Q = 512 * 1024;
constexpr size_t WS_PML = WS_Q + 16384;
constexpr size_t WS_PO = WS_PML + 2048;
constexpr size_t WS_GU = 1u << 20;
constexpr size_t WS_DT = 5u << 20;

__device__ __forceinline__ void ln8(const u32x4 xr, float (&v)[8]) {
#pragma unroll
  for (int k = 0; k < 4; ++k) { v[2 * k] = lo16(xr[k]); v[2 * k + 1] = hi16(xr[k]); }
  float s = ((v[0] + v[1]) + (v[2] + v[3])) + ((v[4] + v[5]) + (v[6] + v[7]));
  s = wave_sum(s);
  const float mean = s * (1.0f / 512.0f);
  float ss = 0.f;
#pragma unroll
  for (int i = 0; i < 8; ++i) { const float d = v[i] - mean; ss += d * d; }
  ss = wave_sum(ss);
  const float var = rbf(ss * (1.0f / 512.0f)), mb = rbf(mean);
  const float den = rbf(sqrtf(rbf(var + 1e-5f)));
#pragma unroll
  for (int i = 0; i < 8; ++i) v[i] = rbf(rbf(v[i] - mb) / den);
}

__global__ void __launch_bounds__(NTHR, 2) k_attn(const unsigned short* __restrict__ x, const unsigned short* __restrict__ kc,
                                                  const unsigned short* __restrict__ vc, const unsigned short* __restrict__ wq,
                                                  unsigned char* __restrict__ ws) {
  __shared__ __attribute__((aligned(16))) unsigned char smem[16384 + 4096 + NWAVE * 132 * 4];
  float* red = (float*)(smem + 16384);
  float* red2 = (float*)(smem + 16384 + 4096);
  const int tid = threadIdx.x, lane = tid & 63, wave = __builtin_amdgcn_readfirstlane(tid >> 6), r = lane >> 4, c = lane & 15;
  const int bid = blockIdx.x;
  const int b = bid >> 7, h = (bid >> 2) & 31, sp = bid & 3;
  STAMP(0, 0);
  const u32x4 xr = *(const u32x4*)(x + b * DM + lane * 8);
  const int rg = tid >> 4;
  u32x4 w[16];
  {
    const u32x4* wp = (const u32x4*)(wq + (size_t)h * DH) + c;
#pragma unroll
    for (int i = 0; i < 16; ++i) w[i] = wp[(size_t)(rg + 32 * i) * (NH * DH / 8)];
  }
  __builtin_amdgcn_sched_barrier(0);
  const size_t base = ((size_t)(b * NH + h) * SK + (size_t)sp * KPS) * DH;
  const u32x4* kp = (const u32x4*)(kc + base) + wave * 256 + r * 16 + c;
  const u32x4* vp = (const u32x4*)(vc + base) + wave * 256 + r * 16 + c;
  constexpr int DEPTH = 4;
  u32x4 Kb[DEPTH][4], Vb[DEPTH][4];
#pragma unroll
  for (int t = 0; t < 4; ++t) {
    Kb[0][t] = __builtin_nontemporal_load(kp + t * 64);
    Vb[0][t] = __builtin_nontemporal_load(vp + t * 64);
  }
  __builtin_amdgcn_sched_barrier(0);
  float* lnw = (float*)smem + wave * 512;
  {
    float v[8];
    ln8(xr, v);
    *(f32x4*)(lnw + lane * 8) = f32x4{v[0], v[1], v[2], v[3]};
    *(f32x4*)(lnw + lane * 8 + 4) = f32x4{v[4], v[5], v[6], v[7]};
  }
  asm volatile("s_waitcnt lgkmcnt(0)" ::: "memory");
  {
    float a[8];
#pragma unroll
    for (int k = 0; k < 8; ++k) a[k] = 0.f;
#pragma unroll
    for (int i = 0; i < 16; ++i) {
      const float xl = lnw[rg + 32 * i];
#pragma unroll
      for (int k = 0; k < 4; ++k) { a[2 * k] = fmaf(xl, lo16(w[i][k]), a[2 * k]); a[2 * k + 1] = fmaf(xl, hi16(w[i][k]), a[2 * k + 1]); }
    }
#pragma unroll
    for (int k = 0; k < 8; ++k) a[k] = xrow_sum(a[k]);
    if (r == 0) {
      *(f32x4*)(red + wave * 128 + c * 8) = f32x4{a[0], a[1], a[2], a[3]};
      *(f32x4*)(red + wave * 128 + c * 8 + 4) = f32x4{a[4], a[5], a[6], a[7]};
    }
  }
  __syncthreads();
  u32x4 q;
  {
    f32x4 s0 = {0, 0, 0, 0}, s1 = {0, 0, 0, 0};
#pragma unroll
    for (int g = 0; g < 8; ++g) { s0 += *(const f32x4*)(red + g * 128 + c * 8); s1 += *(const f32x4*)(red + g * 128 + c * 8 + 4); }
    q[0] = (unsigned)f2bf(s0[0]) | ((unsigned)f2bf(s0[1]) << 16); q[1] = (unsigned)f2bf(s0[2]) | ((unsigned)f2bf(s0[3]) << 16);
    q[2] = (unsigned)f2bf(s1[0]) | ((unsigned)f2bf(s1[1]) << 16); q[3] = (unsigned)f2bf(s1[2]) | ((unsigned)f2bf(s1[3]) << 16);
  }
  STAMP(0, 1);
#pragma unroll
  for (int d = 1; d < DEPTH; ++d)
#pragma unroll
    for (int t = 0; t < 4; ++t) {
      Kb[d][t] = __builtin_nontemporal_load(kp + d * 2048 + t * 64);
      Vb[d][t] = __builtin_nontemporal_load(vp + d * 2048 + t * 64);
    }
  __builtin_amdgcn_sched_barrier(0);
  {
    const int i = bid * 512 + tid;
    if (i < (int)(WS_GEND / 16)) ((u32x4*)ws)[i] = u32x4{0u, 0u, 0u, 0u};
  }
  __builtin_amdgcn_sched_barrier(0);
  STAMP(0, 5);
  constexpr float SCALE = 0.08838834764831845f, L2E = 1.4426950408889634f;
  float m = -INFINITY, l = 0.f, acc[8];
#pragma unroll
  for (int k = 0; k < 8; ++k) acc[k] = 0.f;
  constexpr int NIT = NPAGE / DEPTH;
  for (int it = 0; it < NIT; ++it) {
    const bool more = it + 1 < NIT;
    const int off = more ? (it + 1) * DEPTH * 2048 : -(wave * 256 + r * 16 + c);
    const int sd = more ? 2048 : 0, st = more ? 64 : 0;
#pragma unroll
    for (int d = 0; d < DEPTH; ++d) {
      float s[4];
#pragma unroll
      for (int t = 0; t < 4; ++t) {
        float dd = 0.f;
#pragma unroll
        for (int k = 0; k < 4; ++k) dd = dot2(Kb[d][t][k], q[k], dd);
        dd = row16_sum(dd);
        s[t] = rbf(rbf(dd) * SCALE) * L2E;
      }
      const float pm = fmaxf(fmaxf(s[0], s[1]), fmaxf(s[2], s[3]));
      const float mn = fmaxf(m, pm);
      const float alpha = __builtin_amdgcn_exp2f(m - mn);
      m = mn;
      float pp[4];
#pragma unroll
      for (int t = 0; t < 4; ++t) pp[t] = __builtin_amdgcn_exp2f(s[t] - mn);
      l = fmaf(l, alpha, (pp[0] + pp[1]) + (pp[2] + pp[3]));
#pragma unroll
      for (int k = 0; k < 8; ++k) acc[k] *= alpha;
#pragma unroll
      for (int t = 0; t < 4; ++t)
#pragma unroll
        for (int k = 0; k < 4; ++k) {
          acc[2 * k] = fmaf(pp[t], lo16(Vb[d][t][k]), acc[2 * k]);
          acc[2 * k + 1] = fmaf(pp[t], hi16(Vb[d][t][k]), acc[2 * k + 1]);
        }
      asm volatile("" : "+v"(acc[0]), "+v"(acc[1]), "+v"(acc[2]), "+v"(acc[3]), "+v"(acc[4]), "+v"(acc[5]), "+v"(acc[6]), "+v"(acc[7]));
      __builtin_amdgcn_sched_barrier(0);
#pragma unroll
      for (int t = 0; t < 4; ++t) {
        Kb[d][t] = __builtin_nontemporal_load(kp + off + d * sd + t * st);
        Vb[d][t] = __builtin_nontemporal_load(vp + off + d * sd + t * st);
      }
      __builtin_amdgcn_sched_barrier(0);
    }
    __builtin_amdgcn_s_barrier();
  }
  STAMP(0, 2);
  {
    const float M = xrow_max(m);
    const float wgt = __builtin_amdgcn_exp2f(m - M);
    const float L = xrow_sum(l * wgt);
#pragma unroll
    for (int k = 0; k < 8; ++k) acc[k] = xrow_sum(acc[k] * wgt);
    float* rec = red2 + wave * 132;
    if (r == 0) {
      *(f32x4*)(rec + 4 + 8 * c) = f32x4{acc[0], acc[1], acc[2], acc[3]};
      *(f32x4*)(rec + 8 + 8 * c) = f32x4{acc[4], acc[5], acc[6], acc[7]};
      if (c == 0) { rec[0] = M; rec[1] = L; }
    }
  }
  __syncthreads();
  if (tid < DH) {
    float M = -INFINITY;
#pragma unroll
    for (int g = 0; g < NWAVE; ++g) M = fmaxf(M, red2[g * 132]);
    float L = 0.f, o = 0.f;
#pragma unroll
    for (int g = 0; g < NWAVE; ++g) {
      const float wgt = __builtin_amdgcn_exp2f(red2[g * 132] - M);
      L = fmaf(wgt, red2[g * 132 + 1], L);
      o = fmaf(wgt, red2[g * 132 + 4 + tid], o);
    }
    ((float*)(ws + WS_PO))[(size_t)bid * DH + tid] = o;
    if (tid == 0) ((float2*)(ws + WS_PML))[bid] = make_float2(M, L);
  }
  STAMP(0, 3);
}

#define RLX_AGENT __ATOMIC_RELAXED, __HIP_MEMORY_SCOPE_AGENT
constexpr unsigned SPIN_CAP = 1u << 22;

constexpr int TAIL_BLOCKS = 128 + 112 + 64;
__global__ void __launch_bounds__(256) k_tail(const unsigned short* __restrict__ x, const unsigned short* __restrict__ wo,
                                              const unsigned short* __restrict__ wg, const unsigned short* __restrict__ wu,
                                              const unsigned short* __restrict__ wd, unsigned char* ws, unsigned short* __restrict__ out) {
  __shared__ __attribute__((aligned(16))) float hs[NB * DM];
  __shared__ __attribute__((aligned(16))) float red[4 * 256];
  __shared__ __attribute__((aligned(16))) float os[NB * DH];
  const int tid = threadIdx.x, lane = tid & 63, wave = tid >> 6, bid = blockIdx.x;
  u64* g0 = (u64*)(ws + WS_G0);
  u64* g1 = (u64*)(ws + WS_G1);
  u64* g2 = (u64*)(ws + WS_G2);
  if (bid < 128) {
    const int j = bid;
    STAMP(1, 0);
    const int h = j >> 2, cq = j & 3;
    const int sb = tid >> 7, sd = tid & 127;
    const int p0 = (sb * NH + h) * NSPLIT;
    const f32x4 ml0 = *(const f32x4*)(ws + WS_PML + (size_t)p0 * 8), ml1 = *(const f32x4*)(ws + WS_PML + (size_t)p0 * 8 + 16);
    float po[4];
#pragma unroll
    for (int s = 0; s < 4; ++s) po[s] = ((const float*)(ws + WS_PO))[(size_t)(p0 + s) * DH + sd];
    const int cg = tid & 15, rg0 = tid >> 4;
    u32x4 wv[8];
#pragma unroll
    for (int i = 0; i < 8; ++i) wv[i] = *(const u32x4*)(wo + (size_t)(h * DH + rg0 + 16 * i) * DM + cq * 128 + cg * 8);
    const unsigned short xr = x[((tid >> 3) & 1) * DM + (j & 63) * 8 + (tid & 7)];
    __builtin_amdgcn_sched_barrier(0);
    {
      const float M = fmaxf(fmaxf(ml0[0], ml0[2]), fmaxf(ml1[0], ml1[2]));
      const float w0 = __builtin_amdgcn_exp2f(ml0[0] - M), w1 = __builtin_amdgcn_exp2f(ml0[2] - M);
      const float w2 = __builtin_amdgcn_exp2f(ml1[0] - M), w3 = __builtin_amdgcn_exp2f(ml1[2] - M);
      const float L = fmaf(w3, ml1[3], fmaf(w2, ml1[1], fmaf(w1, ml0[3], w0 * ml0[1])));
      const float o = fmaf(w3, po[3], fmaf(w2, po[2], fmaf(w1, po[1], w0 * po[0])));
      os[tid] = rbf(o / L);
    }
    __syncthreads();
    {
      float a[2][8];
#pragma unroll
      for (int k = 0; k < 8; ++k) { a[0][k] = 0.f; a[1][k] = 0.f; }
#pragma unroll
      for (int i = 0; i < 8; ++i) {
        const float o0 = os[rg0 + 16 * i], o1 = os[DH + rg0 + 16 * i];
#pragma unroll
        for (int k = 0; k < 4; ++k) {
          a[0][2 * k] = fmaf(o0, lo16(wv[i][k]), a[0][2 * k]); a[0][2 * k + 1] = fmaf(o0, hi16(wv[i][k]), a[0][2 * k + 1]);
          a[1][2 * k] = fmaf(o1, lo16(wv[i][k]), a[1][2 * k]); a[1][2 * k + 1] = fmaf(o1, hi16(wv[i][k]), a[1][2 * k + 1]);
        }
      }
#pragma unroll
      for (int bb = 0; bb < 2; ++bb)
#pragma unroll
        for (int k = 0; k < 8; ++k) a[bb][k] = xrow_sum(a[bb][k]);
      if (lane < 16) {
#pragma unroll
        for (int bb = 0; bb < 2; ++bb) {
          *(f32x4*)(red + wave * 256 + bb * 128 + lane * 8) = f32x4{a[bb][0], a[bb][1], a[bb][2], a[bb][3]};
          *(f32x4*)(red + wave * 256 + bb * 128 + lane * 8 + 4) = f32x4{a[bb][4], a[bb][5], a[bb][6], a[bb][7]};
        }
      }
    }
    __syncthreads();
    {
      const float y = (red[tid] + red[256 + tid]) + (red[512 + tid] + red[768 + tid]);
      const int col = cq * 128 + sd;
      __hip_atomic_store(g0 + (((size_t)(col >> 3) * NH + h) * 2 + sb) * 8 + (col & 7), (1ull << 32) | (u64)__float_as_uint(y), RLX_AGENT);
    }
    STAMP(1, 6);
    if (j < 64) {
      u64 a0, a1; unsigned spins = 0;
      do {
        a0 = __hip_atomic_load(g0 + (size_t)j * 512 + tid, RLX_AGENT);
        a1 = __hip_atomic_load(g0 + (size_t)j * 512 + 256 + tid, RLX_AGENT);
      } while ((((a0 & a1) >> 32) != 1ull) && ++spins < SPIN_CAP);
      float s = __uint_as_float((unsigned)a0) + __uint_as_float((unsigned)a1);
      s = xrow_sum(s);
      if (lane < 16) os[wave * 16 + lane] = s;
      __syncthreads();
      if (tid < 16) {
        const float t = (os[tid] + os[16 + tid]) + (os[32 + tid] + os[48 + tid]);
        const float hv = rbf(bf2f(xr) + rbf(t));
        const float other = __builtin_bit_cast(float, __builtin_amdgcn_mov_dpp(__builtin_bit_cast(int, hv), XOR8, 0xf, 0xf, true));
        if (tid < 8) __hip_atomic_store(g1 + j * 8 + tid, (1ull << 32) | (__float_as_uint(other) & 0xffff0000u) | (__float_as_uint(hv) >> 16), RLX_AGENT);
      }
      STAMP(1, 1);
    }
  } else if (bid < 240) {
    const int e = bid - 128, j = e < 96 ? ((e >> 5) * 8 + (e & 7)) * 4 + ((e >> 3) & 3) : (24 + ((e - 96) & 3)) * 4 + ((e - 96) >> 2);
    const int ch = tid & 1, rg = tid >> 1;
    u32x4 g[4], u[4];
#pragma unroll
    for (int i = 0; i < 4; ++i) {
      g[i] = *(const u32x4*)(wg + (size_t)(rg + 128 * i) * FF + j * 16 + ch * 8);
      u[i] = *(const u32x4*)(wu + (size_t)(rg + 128 * i) * FF + j * 16 + ch * 8);
    }
    __builtin_amdgcn_sched_barrier(0);
    {
      u64 a0, a1; unsigned spins = 0;
      do {
        a0 = __hip_atomic_load(g1 + tid, RLX_AGENT);
        a1 = __hip_atomic_load(g1 + 256 + tid, RLX_AGENT);
      } while ((((a0 & a1) >> 32) != 1ull) && ++spins < SPIN_CAP);
      hs[tid] = lo16((unsigned)a0); hs[DM + tid] = hi16((unsigned)a0);
      hs[256 + tid] = lo16((unsigned)a1); hs[DM + 256 + tid] = hi16((unsigned)a1);
    }
    __syncthreads();
    STAMP(1, 2);
    float mb0, mb1, den0, den1;
    {
      float v0[8], v1[8], s0 = 0.f, s1 = 0.f;
#pragma unroll
      for (int i = 0; i < 8; ++i) { v0[i] = hs[lane + 64 * i]; v1[i] = hs[DM + lane + 64 * i]; s0 += v0[i]; s1 += v1[i]; }
      s0 = wave_sum(s0); s1 = wave_sum(s1);
      const float mean0 = s0 * (1.0f / 512.0f), mean1 = s1 * (1.0f / 512.0f);
      float q0 = 0.f, q1 = 0.f;
#pragma unroll
      for (int i = 0; i < 8; ++i) { const float d0 = v0[i] - mean0, d1 = v1[i] - mean1; q0 += d0 * d0; q1 += d1 * d1; }
      q0 = wave_sum(q0); q1 = wave_sum(q1);
      mb0 = rbf(mean0); mb1 = rbf(mean1);
      den0 = rbf(sqrtf(rbf(rbf(q0 * (1.0f / 512.0f)) + 1e-5f)));
      den1 = rbf(sqrtf(rbf(rbf(q1 * (1.0f / 512.0f)) + 1e-5f)));
    }
    float a[4][8];
#pragma unroll
    for (int qq = 0; qq < 4; ++qq)
#pragma unroll
      for (int k = 0; k < 8; ++k) a[qq][k] = 0.f;
#pragma unroll
    for (int i = 0; i < 4; ++i) {
      const float x0 = rbf(rbf(hs[rg + 128 * i] - mb0) / den0), x1 = rbf(rbf(hs[DM + rg + 128 * i] - mb1) / den1);
#pragma unroll
      for (int k = 0; k < 4; ++k) {
        a[0][2 * k] = fmaf(x0, lo16(g[i][k]), a[0][2 * k]); a[0][2 * k + 1] = fmaf(x0, hi16(g[i][k]), a[0][2 * k + 1]);
        a[1][2 * k] = fmaf(x0, lo16(u[i][k]), a[1][2 * k]); a[1][2 * k + 1] = fmaf(x0, hi16(u[i][k]), a[1][2 * k + 1]);
        a[2][2 * k] = fmaf(x1, lo16(g[i][k]), a[2][2 * k]); a[2][2 * k + 1] = fmaf(x1, hi16(g[i][k]), a[2][2 * k + 1]);
        a[3][2 * k] = fmaf(x1, lo16(u[i][k]), a[3][2 * k]); a[3][2 * k + 1] = fmaf(x1, hi16(u[i][k]), a[3][2 * k + 1]);
      }
    }
#pragma unroll
    for (int qq = 0; qq < 4; ++qq)
#pragma unroll
      for (int k = 0; k < 8; ++k) {
        float v = a[qq][k];
        v += dpp<XOR2>(v); v += dpp<ROR4>(v); v += dpp<XOR8>(v);
        a[qq][k] = xrow_sum(v);
      }
    if (lane < 2) {
#pragma unroll
      for (int qq = 0; qq < 4; ++qq)
#pragma unroll
        for (int k = 0; k < 8; ++k) red[wave * 64 + qq * 16 + lane * 8 + k] = a[qq][k];
    }
    __syncthreads();
    if (tid < 16) {
      float g0v = 0.f, u0 = 0.f, g1v = 0.f, u1 = 0.f;
#pragma unroll
      for (int w2 = 0; w2 < 4; ++w2) { g0v += red[w2 * 64 + tid]; u0 += red[w2 * 64 + 16 + tid]; g1v += red[w2 * 64 + 32 + tid]; u1 += red[w2 * 64 + 48 + tid]; }
      g0v = rbf(g0v); u0 = rbf(u0); g1v = rbf(g1v); u1 = rbf(u1);
      const unsigned short a0 = f2bf(rbf(g0v / (1.0f + __expf(-g0v))) * u0), a1 = f2bf(rbf(g1v / (1.0f + __expf(-g1v))) * u1);
      __hip_atomic_store(g2 + j * 16 + tid, (1ull << 32) | ((unsigned)a1 << 16) | (unsigned)a0, RLX_AGENT);
    }
    STAMP(1, 3);
  } else {
    const int e = bid - 240, j = (e & 7) * 8 + (e >> 3);
    u32x4 d[7];
#pragma unroll
    for (int i = 0; i < 7; ++i) d[i] = *(const u32x4*)(wd + (size_t)(tid + 256 * i) * DM + j * 8);
    __builtin_amdgcn_sched_barrier(0);
    u64 av[7], hv; unsigned spins = 0; bool ok;
    do {
      ok = true;
#pragma unroll
      for (int i = 0; i < 7; ++i) { av[i] = __hip_atomic_load(g2 + tid + 256 * i, RLX_AGENT); }
      hv = __hip_atomic_load(g1 + j * 8 + (tid & 7), RLX_AGENT);
#pragma unroll
      for (int i = 0; i < 7; ++i) ok &= (av[i] >> 32) == 1ull;
      ok &= (hv >> 32) == 1ull;
    } while (!ok && ++spins < SPIN_CAP);
    float a[2][8];
#pragma unroll
    for (int k = 0; k < 8; ++k) { a[0][k] = 0.f; a[1][k] = 0.f; }
#pragma unroll
    for (int i = 0; i < 7; ++i) {
      const float a0 = lo16((unsigned)av[i]), a1 = hi16((unsigned)av[i]);
#pragma unroll
      for (int k = 0; k < 4; ++k) {
        a[0][2 * k] = fmaf(a0, lo16(d[i][k]), a[0][2 * k]); a[0][2 * k + 1] = fmaf(a0, hi16(d[i][k]), a[0][2 * k + 1]);
        a[1][2 * k] = fmaf(a1, lo16(d[i][k]), a[1][2 * k]); a[1][2 * k + 1] = fmaf(a1, hi16(d[i][k]), a[1][2 * k + 1]);
      }
    }
#pragma unroll
    for (int qq = 0; qq < 2; ++qq)
#pragma unroll
      for (int k = 0; k < 8; ++k) a[qq][k] = wave_sum(a[qq][k]);
    if (lane == 0) {
#pragma unroll
      for (int qq = 0; qq < 2; ++qq)
#pragma unroll
        for (int k = 0; k < 8; ++k) red[wave * 16 + qq * 8 + k] = a[qq][k];
    }
    __syncthreads();
    if (tid < 16) {
      const float s = (red[tid] + red[16 + tid]) + (red[32 + tid] + red[48 + tid]);
      const float hres = (tid >> 3) ? hi16((unsigned)hv) : lo16((unsigned)hv);
      out[(tid >> 3) * DM + j * 8 + (tid & 7)] = f2bf(hres + rbf(s));
    }
  }
}

extern "C" void kernel_launch(void* const* d_in, const int* in_sizes, int n_in,
                              void* d_out, int out_size, void* d_ws, size_t ws_size,
                              hipStream_t stream) {
  const unsigned short* x = (const unsigned short*)d_in[0];
  const unsigned short* kc = (const unsigned short*)d_in[1];
  const unsigned short* vc = (const unsigned short*)d_in[2];
  const unsigned short* wq = (const unsigned short*)d_in[3];
  const unsigned short* wo = (const unsigned short*)d_in[4];
  const unsigned short* wg = (const unsigned short*)d_in[6];
  const unsigned short* wu = (const unsigned short*)d_in[7];
  const unsigned short* wd = (const unsigned short*)d_in[8];
  unsigned char* ws = (unsigned char*)d_ws;
  k_attn<<<256, NTHR, 0, stream>>>(x, kc, vc, wq, ws);
  k_tail<<<TAIL_BLOCKS, 256, 0, stream>>>(x, wo, wg, wu, wd, ws, (unsigned short*)d_out);
}
```

```cpp
#define PRE 2
#define LBAR 1
#define DEP 4
#define PASS0 1
#define S2_DELAY 2
#include <hip/hip_runtime.h>
#include <hip/hip_bf16.h>
#include <stdint.h>

typedef unsigned u32x4 __attribute__((ext_vector_type(4)));
typedef float f32x4 __attribute__((ext_vector_type(4)));
typedef __bf16 bf16x2 __attribute__((ext_vector_type(2)));
typedef unsigned long long u64;

constexpr int NB = 2, DM = 512, NH = 32, SK = 16384, DH = 128, FF = 1792;
constexpr int NSPLIT = 4;
constexpr int KPS = SK / NSPLIT;
constexpr int NWAVE = 8, NTHR = 512;
constexpr int PAGE = 16;
constexpr int NPAGE = KPS / NWAVE / PAGE;
constexpr int NPART = NB * NH * NSPLIT;

__device__ __forceinline__ float bf2f(unsigned short b) { return __builtin_bit_cast(float, (unsigned)b << 16); }
__device__ __forceinline__ float rbf(float x) { return __bfloat162float(__float2bfloat16(x)); }
__device__ __forceinline__ unsigned short f2bf(float x) { return __builtin_bit_cast(unsigned short, __float2bfloat16(x)); }
__device__ __forceinline__ float lo16(unsigned u) { return __builtin_bit_cast(float, u << 16); }
__device__ __forceinline__ float hi16(unsigned u) { return __builtin_bit_cast(float, u & 0xffff0000u); }
__device__ __forceinline__ float dot2(unsigned k, unsigned q, float acc) {
  return __builtin_amdgcn_fdot2_f32_bf16(__builtin_bit_cast(bf16x2, k), __builtin_bit_cast(bf16x2, q), acc, false);
}
template <int CTRL> __device__ __forceinline__ float dpp(float x) {
  return __builtin_bit_cast(float, __builtin_amdgcn_mov_dpp(__builtin_bit_cast(int, x), CTRL, 0xf, 0xf, true));
}
constexpr int XOR1 = 0xB1, XOR2 = 0x4E, XOR7 = 0x141, XOR8 = 0x128, ROR4 = 0x124;
__device__ __forceinline__ float row16_sum(float x) {
  x += dpp<XOR1>(x); x += dpp<XOR2>(x); x += dpp<XOR7>(x); x += dpp<XOR8>(x); return x;
}
__device__ __forceinline__ float xrow_sum(float x) {
  auto s = __builtin_amdgcn_permlane16_swap(__float_as_uint(x), __float_as_uint(x), false, false);
  x = __uint_as_float(s[0]) + __uint_as_float(s[1]);
  auto t = __builtin_amdgcn_permlane32_swap(__float_as_uint(x), __float_as_uint(x), false, false);
  return __uint_as_float(t[0]) + __uint_as_float(t[1]);
}
__device__ __forceinline__ float xrow_max(float x) {
  auto s = __builtin_amdgcn_permlane16_swap(__float_as_uint(x), __float_as_uint(x), false, false);
  x = fmaxf(__uint_as_float(s[0]), __uint_as_float(s[1]));
  auto t = __builtin_amdgcn_permlane32_swap(__float_as_uint(x), __float_as_uint(x), false, false);
  return fmaxf(__uint_as_float(t[0]), __uint_as_float(t[1]));
}
__device__ __forceinline__ float wave_sum(float x) { return xrow_sum(row16_sum(x)); }

#define STAMP(k, i)
constexpr size_t WS_G0 = 0;
constexpr size_t WS_G1 = 262144;
constexpr size_t WS_G2 = WS_G1 + 4096;
constexpr size_t WS_GEND = WS_G2 + FF * 8;
constexpr size_t WS_Q = 512 * 1024;
constexpr size_t WS_PML = WS_Q + 16384;
constexpr size_t WS_PO = WS_PML + 2048;
constexpr size_t WS_GU = 1u << 20;
constexpr size_t WS_DT = 5u << 20;

__device__ __forceinline__ void ln8(const u32x4 xr, float (&v)[8]) {
#pragma unroll
  for (int k = 0; k < 4; ++k) { v[2 * k] = lo16(xr[k]); v[2 * k + 1] = hi16(xr[k]); }
  float s = ((v[0] + v[1]) + (v[2] + v[3])) + ((v[4] + v[5]) + (v[6] + v[7]));
  s = wave_sum(s);
  const float mean = s * (1.0f / 512.0f);
  float ss = 0.f;
#pragma unroll
  for (int i = 0; i < 8; ++i) { const float d = v[i] - mean; ss += d * d; }
  ss = wave_sum(ss);
  const float var = rbf(ss * (1.0f / 512.0f)), mb = rbf(mean);
  const float den = rbf(sqrtf(rbf(var + 1e-5f)));
#pragma unroll
  for (int i = 0; i < 8; ++i) v[i] = rbf(rbf(v[i] - mb) / den);
}

__global__ void __launch_bounds__(NTHR, 2) k_attn(const unsigned short* __restrict__ x, const unsigned short* __restrict__ kc,
                                                  const unsigned short* __restrict__ vc, const unsigned short* __restrict__ wq,
                                                  unsigned char* __restrict__ ws) {
  __shared__ __attribute__((aligned(16))) unsigned char smem[16384 + 4096 + NWAVE * 132 * 4];
  float* red = (float*)(smem + 16384);
  float* red2 = (float*)(smem + 16384 + 4096);
  const int tid = threadIdx.x, lane = tid & 63, wave = __builtin_amdgcn_readfirstlane(tid >> 6), r = lane >> 4, c = lane & 15;
  const int bid = blockIdx.x;
  const int b = bid >> 7, h = (bid >> 2) & 31, sp = bid & 3;
  STAMP(0, 0);
  const u32x4 xr = *(const u32x4*)(x + b * DM + lane * 8);
  const int rg = tid >> 4;
  u32x4 w[16];
  {
    const u32x4* wp = (const u32x4*)(wq + (size_t)h * DH) + c;
#pragma unroll
    for (int i = 0; i < 16; ++i) w[i] = wp[(size_t)(rg + 32 * i) * (NH * DH / 8)];
  }
  __builtin_amdgcn_sched_barrier(0);
  const size_t base = ((size_t)(b * NH + h) * SK + (size_t)sp * KPS) * DH;
  const u32x4* kp = (const u32x4*)(kc + base) + wave * 256 + r * 16 + c;
  const u32x4* vp = (const u32x4*)(vc + base) + wave * 256 + r * 16 + c;
  constexpr int DEPTH = DEP;
  u32x4 Kb[DEPTH][4], Vb[DEPTH][4];
#pragma unroll
  for (int d = 0; d < PRE; ++d)
#pragma unroll
    for (int t = 0; t < 4; ++t) {
      Kb[d][t] = __builtin_nontemporal_load(kp + d * 2048 + t * 64);
      Vb[d][t] = __builtin_nontemporal_load(vp + d * 2048 + t * 64);
    }
  __builtin_amdgcn_sched_barrier(0);
  float* lnw = (float*)smem + wave * 512;
  {
    float v[8];
    ln8(xr, v);
    *(f32x4*)(lnw + lane * 8) = f32x4{v[0], v[1], v[2], v[3]};
    *(f32x4*)(lnw + lane * 8 + 4) = f32x4{v[4], v[5], v[6], v[7]};
  }
  asm volatile("s_waitcnt lgkmcnt(0)" ::: "memory");
  {
    float a[8];
#pragma unroll
    for (int k = 0; k < 8; ++k) a[k] = 0.f;
#pragma unroll
    for (int i = 0; i < 16; ++i) {
      const float xl = lnw[rg + 32 * i];
#pragma unroll
      for (int k = 0; k < 4; ++k) { a[2 * k] = fmaf(xl, lo16(w[i][k]), a[2 * k]); a[2 * k + 1] = fmaf(xl, hi16(w[i][k]), a[2 * k + 1]); }
    }
#pragma unroll
    for (int k = 0; k < 8; ++k) a[k] = xrow_sum(a[k]);
    if (r == 0) {
      *(f32x4*)(red + wave * 128 + c * 8) = f32x4{a[0], a[1], a[2], a[3]};
      *(f32x4*)(red + wave * 128 + c * 8 + 4) = f32x4{a[4], a[5], a[6], a[7]};
    }
  }
  __syncthreads();
  u32x4 q;
  {
    f32x4 s0 = {0, 0, 0, 0}, s1 = {0, 0, 0, 0};
#pragma unroll
    for (int g = 0; g < 8; ++g) { s0 += *(const f32x4*)(red + g * 128 + c * 8); s1 += *(const f32x4*)(red + g * 128 + c * 8 + 4); }
    q[0] = (unsigned)f2bf(s0[0]) | ((unsigned)f2bf(s0[1]) << 16); q[1] = (unsigned)f2bf(s0[2]) | ((unsigned)f2bf(s0[3]) << 16);
    q[2] = (unsigned)f2bf(s1[0]) | ((unsigned)f2bf(s1[1]) << 16); q[3] = (unsigned)f2bf(s1[2]) | ((unsigned)f2bf(s1[3]) << 16);
  }
  STAMP(0, 1);
#pragma unroll
  for (int d = PRE; d < DEPTH; ++d)
#pragma unroll
    for (int t = 0; t < 4; ++t) {
      Kb[d][t] = __builtin_nontemporal_load(kp + d * 2048 + t * 64);
      Vb[d][t] = __builtin_nontemporal_load(vp + d * 2048 + t * 64);
    }
  __builtin_amdgcn_sched_barrier(0);
  {
    const int i = bid * 512 + tid;
    if (i < (int)(WS_GEND / 16)) ((u32x4*)ws)[i] = u32x4{0u, 0u, 0u, 0u};
  }
  __builtin_amdgcn_sched_barrier(0);
  STAMP(0, 5);
  constexpr float SCALE = 0.08838834764831845f, L2E = 1.4426950408889634f;
  float m = -INFINITY, l = 0.f, acc[8];
#pragma unroll
  for (int k = 0; k < 8; ++k) acc[k] = 0.f;
  constexpr int NIT = NPAGE / DEPTH;
  for (int it = 0; it < NIT; ++it) {
    const bool more = it + 1 < NIT;
    const int off = more ? (it + 1) * DEPTH * 2048 : -(wave * 256 + r * 16 + c);
    const int sd = more ? 2048 : 0, st = more ? 64 : 0;
#pragma unroll
    for (int d = 0; d < DEPTH; ++d) {
      float s[4];
#pragma unroll
      for (int t = 0; t < 4; ++t) {
        float dd = 0.f;
#pragma unroll
        for (int k = 0; k < 4; ++k) dd = dot2(Kb[d][t][k], q[k], dd);
        dd = row16_sum(dd);
        s[t] = rbf(rbf(dd) * SCALE) * L2E;
      }
      const float pm = fmaxf(fmaxf(s[0], s[1]), fmaxf(s[2], s[3]));
      const float mn = fmaxf(m, pm);
      const float alpha = __builtin_amdgcn_exp2f(m - mn);
      m = mn;
      float pp[4];
#pragma unroll
      for (int t = 0; t < 4; ++t) pp[t] = __builtin_amdgcn_exp2f(s[t] - mn);
      l = fmaf(l, alpha, (pp[0] + pp[1]) + (pp[2] + pp[3]));
#pragma unroll
      for (int k = 0; k < 8; ++k) acc[k] *= alpha;
#pragma unroll
      for (int t = 0; t < 4; ++t)
#pragma unroll
        for (int k = 0; k < 4; ++k) {
          acc[2 * k] = fmaf(pp[t], lo16(Vb[d][t][k]), acc[2 * k]);
          acc[2 * k + 1] = fmaf(pp[t], hi16(Vb[d][t][k]), acc[2 * k + 1]);
        }
      asm volatile("" : "+v"(acc[0]), "+v"(acc[1]), "+v"(acc[2]), "+v"(acc[3]), "+v"(acc[4]), "+v"(acc[5]), "+v"(acc[6]), "+v"(acc[7]));
      __builtin_amdgcn_sched_barrier(0);
#pragma unroll
      for (int t = 0; t < 4; ++t) {
        Kb[d][t] = __builtin_nontemporal_load(kp + off + d * sd + t * st);
        Vb[d][t] = __builtin_nontemporal_load(vp + off + d * sd + t * st);
      }
      __builtin_amdgcn_sched_barrier(0);
    }
    __builtin_amdgcn_s_barrier();
  }
  STAMP(0, 2);
  {
    const float M = xrow_max(m);
    const float wgt = __builtin_amdgcn_exp2f(m - M);
    const float L = xrow_sum(l * wgt);
#pragma unroll
    for (int k = 0; k < 8; ++k) acc[k] = xrow_sum(acc[k] * wgt);
    float* rec = red2 + wave * 132;
    if (r == 0) {
      *(f32x4*)(rec + 4 + 8 * c) = f32x4{acc[0], acc[1], acc[2], acc[3]};
      *(f32x4*)(rec + 8 + 8 * c) = f32x4{acc[4], acc[5], acc[6], acc[7]};
      if (c == 0) { rec[0] = M; rec[1] = L; }
    }
  }
  __syncthreads();
  if (tid < DH) {
    float M = -INFINITY;
#pragma unroll
    for (int g = 0; g < NWAVE; ++g) M = fmaxf(M, red2[g * 132]);
    float L = 0.f, o = 0.f;
#pragma unroll
    for (int g = 0; g < NWAVE; ++g) {
      const float wgt = __builtin_amdgcn_exp2f(red2[g * 132] - M);
      L = fmaf(wgt, red2[g * 132 + 1], L);
      o = fmaf(wgt, red2[g * 132 + 4 + tid], o);
    }
    ((float*)(ws + WS_PO))[(size_t)bid * DH + tid] = o;
    if (tid == 0) ((float2*)(ws + WS_PML))[bid] = make_float2(M, L);
  }
  STAMP(0, 3);
}

#define RLX_AGENT __ATOMIC_RELAXED, __HIP_MEMORY_SCOPE_AGENT
constexpr unsigned SPIN_CAP = 1u << 22;

constexpr int TAIL_BLOCKS = 128 + 112;
#define TSTEP(N, BIT, XCHG)                                                                       \
  _Pragma("unroll") for (int i_ = 0; i_ < (N); ++i_) {                                            \
    const float keep_ = (BIT) ? a[i_ + (N)] : a[i_], send_ = (BIT) ? a[i_] : a[i_ + (N)];         \
    a[i_] = keep_ + XCHG(send_);                                                                  \
  }
__device__ __forceinline__ float x16(float v) {
  auto s = __builtin_amdgcn_permlane16_swap(__float_as_uint(v), __float_as_uint(v), false, false);
  return __uint_as_float((__lane_id() & 16) ? s[0] : s[1]);
}
__device__ __forceinline__ float x32(float v) {
  auto s = __builtin_amdgcn_permlane32_swap(__float_as_uint(v), __float_as_uint(v), false, false);
  return __uint_as_float((__lane_id() & 32) ? s[0] : s[1]);
}
__global__ void __launch_bounds__(256) k_tail(const unsigned short* __restrict__ x, const unsigned short* __restrict__ wo,
                                              const unsigned short* __restrict__ wg, const unsigned short* __restrict__ wu,
                                              const unsigned short* __restrict__ wd, unsigned char* ws, unsigned short* __restrict__ out) {
  __shared__ __attribute__((aligned(16))) float hs[NB * DM];
  __shared__ __attribute__((aligned(16))) float red[4 * 256];
  __shared__ __attribute__((aligned(16))) float os[NB * DH];
  const int tid = threadIdx.x, lane = tid & 63, wave = tid >> 6, bid = blockIdx.x;
  u64* g0 = (u64*)(ws + WS_G0);
  u64* g1 = (u64*)(ws + WS_G1);
  u64* g2 = (u64*)(ws + WS_G2);
  if (bid < 128) {
    const int j = bid;
    STAMP(1, 0);
    const int h = j >> 2, cq = j & 3;
    const int sb = tid >> 7, sd = tid & 127;
    const int p0 = (sb * NH + h) * NSPLIT;
    const f32x4 ml0 = *(const f32x4*)(ws + WS_PML + (size_t)p0 * 8), ml1 = *(const f32x4*)(ws + WS_PML + (size_t)p0 * 8 + 16);
    float po[4];
#pragma unroll
    for (int s = 0; s < 4; ++s) po[s] = ((const float*)(ws + WS_PO))[(size_t)(p0 + s) * DH + sd];
    const int cg = tid & 15, rg0 = tid >> 4;
    u32x4 wv[8];
#pragma unroll
    for (int i = 0; i < 8; ++i) wv[i] = *(const u32x4*)(wo + (size_t)(h * DH + rg0 + 16 * i) * DM + cq * 128 + cg * 8);
    const unsigned short xr = x[((tid >> 3) & 1) * DM + (j & 63) * 8 + (tid & 7)];
    __builtin_amdgcn_sched_barrier(0);
    {
      const float M = fmaxf(fmaxf(ml0[0], ml0[2]), fmaxf(ml1[0], ml1[2]));
      const float w0 = __builtin_amdgcn_exp2f(ml0[0] - M), w1 = __builtin_amdgcn_exp2f(ml0[2] - M);
      const float w2 = __builtin_amdgcn_exp2f(ml1[0] - M), w3 = __builtin_amdgcn_exp2f(ml1[2] - M);
      const float L = fmaf(w3, ml1[3], fmaf(w2, ml1[1], fmaf(w1, ml0[3], w0 * ml0[1])));
      const float o = fmaf(w3, po[3], fmaf(w2, po[2], fmaf(w1, po[1], w0 * po[0])));
      os[tid] = rbf(o / L);
    }
    __syncthreads();
    {
      float a[16];
#pragma unroll
      for (int k = 0; k < 16; ++k) a[k] = 0.f;
#pragma unroll
      for (int i = 0; i < 8; ++i) {
        const float o0 = os[rg0 + 16 * i], o1 = os[DH + rg0 + 16 * i];
#pragma unroll
        for (int k = 0; k < 4; ++k) {
          a[2 * k] = fmaf(o0, lo16(wv[i][k]), a[2 * k]); a[2 * k + 1] = fmaf(o0, hi16(wv[i][k]), a[2 * k + 1]);
          a[8 + 2 * k] = fmaf(o1, lo16(wv[i][k]), a[8 + 2 * k]); a[8 + 2 * k + 1] = fmaf(o1, hi16(wv[i][k]), a[8 + 2 * k + 1]);
        }
      }
      TSTEP(8, lane & 32, x32)
      TSTEP(4, lane & 16, x16)
      *(f32x4*)(red + wave * 256 + (lane >> 5) * 128 + cg * 8 + ((lane >> 4) & 1) * 4) = f32x4{a[0], a[1], a[2], a[3]};
    }
    __syncthreads();
    {
      const float y = (red[tid] + red[256 + tid]) + (red[512 + tid] + red[768 + tid]);
      const int col = cq * 128 + sd;
      __hip_atomic_store(g0 + (((size_t)(col >> 3) * NH + h) * 2 + sb) * 8 + (col & 7), (1ull << 32) | (u64)__float_as_uint(y), RLX_AGENT);
    }
    STAMP(1, 6);
    if (j < 64) {
#pragma unroll 1
      for (int pass = PASS0; pass < 2; ++pass) {
      asm volatile("" : "+s"(pass));
      u64 a0, a1; unsigned spins = 0;
      do {
        a0 = __hip_atomic_load(g0 + (size_t)j * 512 + tid, RLX_AGENT);
        a1 = __hip_atomic_load(g0 + (size_t)j * 512 + 256 + tid, RLX_AGENT);
      } while (pass && (((a0 & a1) >> 32) != 1ull) && ++spins < SPIN_CAP);
      float s = __uint_as_float((unsigned)a0) + __uint_as_float((unsigned)a1);
      s = xrow_sum(s);
      if (lane < 16) os[wave * 16 + lane] = s;
      __syncthreads();
      if (tid < 16) {
        const float t = (os[tid] + os[16 + tid]) + (os[32 + tid] + os[48 + tid]);
        const float hv = rbf(bf2f(xr) + rbf(t));
        const float other = __builtin_bit_cast(float, __builtin_amdgcn_mov_dpp(__builtin_bit_cast(int, hv), XOR8, 0xf, 0xf, true));
        if (pass && tid < 8) __hip_atomic_store(g1 + j * 8 + tid, (1ull << 32) | (__float_as_uint(other) & 0xffff0000u) | (__float_as_uint(hv) >> 16), RLX_AGENT);
      }
      __syncthreads();
      }
      STAMP(1, 1);
    } else {
      const int e = j - 64, jj = (e & 7) * 8 + (e >> 3);
      u32x4 d[7];
#pragma unroll
      for (int i = 0; i < 7; ++i) d[i] = *(const u32x4*)(wd + (size_t)(tid + 256 * i) * DM + jj * 8);
      __builtin_amdgcn_sched_barrier(0);
#pragma unroll 1
      for (int pass = PASS0; pass < 2; ++pass) {
      asm volatile("" : "+s"(pass));
      u64 av[7], hv; unsigned spins = 0; bool ok;
      do {
        ok = true;
#pragma unroll
        for (int i = 0; i < 7; ++i) { av[i] = __hip_atomic_load(g2 + tid + 256 * i, RLX_AGENT); }
        hv = __hip_atomic_load(g1 + jj * 8 + (tid & 7), RLX_AGENT);
#pragma unroll
        for (int i = 0; i < 7; ++i) ok &= (av[i] >> 32) == 1ull;
        ok &= (hv >> 32) == 1ull;
      } while (pass && !ok && ++spins < SPIN_CAP);
      float a[16];
#pragma unroll
      for (int k = 0; k < 16; ++k) a[k] = 0.f;
#pragma unroll
      for (int i = 0; i < 7; ++i) {
        const float a0 = lo16((unsigned)av[i]), a1 = hi16((unsigned)av[i]);
#pragma unroll
        for (int k = 0; k < 4; ++k) {
          a[2 * k] = fmaf(a0, lo16(d[i][k]), a[2 * k]); a[2 * k + 1] = fmaf(a0, hi16(d[i][k]), a[2 * k + 1]);
          a[8 + 2 * k] = fmaf(a1, lo16(d[i][k]), a[8 + 2 * k]); a[8 + 2 * k + 1] = fmaf(a1, hi16(d[i][k]), a[8 + 2 * k + 1]);
        }
      }
      TSTEP(8, lane & 32, x32)
      TSTEP(4, lane & 16, x16)
      TSTEP(2, lane & 8, dpp<XOR8>)
      TSTEP(1, lane & 1, dpp<XOR1>)
      float t = a[0];
      t += dpp<XOR2>(t);
      t += dpp<XOR1>(dpp<XOR7>(t));
      if ((lane & 6) == 0) red[wave * 16 + ((lane >> 5) & 1) * 8 + ((lane >> 4) & 1) * 4 + ((lane >> 3) & 1) * 2 + (lane & 1)] = t;
      __syncthreads();
      if (pass && tid < 16) {
        const float s = (red[tid] + red[16 + tid]) + (red[32 + tid] + red[48 + tid]);
        const float hres = (tid >> 3) ? hi16((unsigned)hv) : lo16((unsigned)hv);
        out[(tid >> 3) * DM + jj * 8 + (tid & 7)] = f2bf(hres + rbf(s));
      }
      __syncthreads();
      }
    }
  } else {
    const int e = bid - 128, j = e < 96 ? ((e >> 5) * 8 + (e & 7)) * 4 + ((e >> 3) & 3) : (24 + ((e - 96) & 3)) * 4 + ((e - 96) >> 2);
    const int ch = lane >> 5, rg = wave * 32 + (lane & 31);
    for (int i = 0; i < S2_DELAY; ++i) __builtin_amdgcn_s_sleep(32);
    u32x4 g[4], u[4];
#pragma unroll
    for (int i = 0; i < 4; ++i) {
      g[i] = *(const u32x4*)(wg + (size_t)(rg + 128 * i) * FF + j * 16 + ch * 8);
      u[i] = *(const u32x4*)(wu + (size_t)(rg + 128 * i) * FF + j * 16 + ch * 8);
    }
    __builtin_amdgcn_sched_barrier(0);
#pragma unroll 1
    for (int pass = PASS0; pass < 2; ++pass) {
    asm volatile("" : "+s"(pass));
    {
      u64 a0, a1; unsigned spins = 0;
      do {
        a0 = __hip_atomic_load(g1 + tid, RLX_AGENT);
        a1 = __hip_atomic_load(g1 + 256 + tid, RLX_AGENT);
      } while (pass && (((a0 & a1) >> 32) != 1ull) && ++spins < SPIN_CAP);
      hs[tid] = lo16((unsigned)a0); hs[DM + tid] = hi16((unsigned)a0);
      hs[256 + tid] = lo16((unsigned)a1); hs[DM + 256 + tid] = hi16((unsigned)a1);
    }
    __syncthreads();
    if (pass) { STAMP(1, 2); }
    float mb0, mb1, den0, den1;
    {
      const f32x4 p0 = *(const f32x4*)(hs + lane * 8), p1 = *(const f32x4*)(hs + lane * 8 + 4);
      const f32x4 r0 = *(const f32x4*)(hs + DM + lane * 8), r1 = *(const f32x4*)(hs + DM + lane * 8 + 4);
      float s0 = ((p0[0] + p0[1]) + (p0[2] + p0[3])) + ((p1[0] + p1[1]) + (p1[2] + p1[3]));
      float s1 = ((r0[0] + r0[1]) + (r0[2] + r0[3])) + ((r1[0] + r1[1]) + (r1[2] + r1[3]));
      s0 = wave_sum(s0); s1 = wave_sum(s1);
      const float mean0 = s0 * (1.0f / 512.0f), mean1 = s1 * (1.0f / 512.0f);
      float q0 = 0.f, q1 = 0.f;
#pragma unroll
      for (int i = 0; i < 4; ++i) {
        float d0 = p0[i] - mean0, d1 = r0[i] - mean1; q0 += d0 * d0; q1 += d1 * d1;
        d0 = p1[i] - mean0; d1 = r1[i] - mean1; q0 += d0 * d0; q1 += d1 * d1;
      }
      q0 = wave_sum(q0); q1 = wave_sum(q1);
      mb0 = rbf(mean0); mb1 = rbf(mean1);
      den0 = rbf(sqrtf(rbf(rbf(q0 * (1.0f / 512.0f)) + 1e-5f)));
      den1 = rbf(sqrtf(rbf(rbf(q1 * (1.0f / 512.0f)) + 1e-5f)));
    }
    float a[32];
#pragma unroll
    for (int k = 0; k < 32; ++k) a[k] = 0.f;
#pragma unroll
    for (int i = 0; i < 4; ++i) {
      const float x0 = rbf(rbf(hs[rg + 128 * i] - mb0) / den0), x1 = rbf(rbf(hs[DM + rg + 128 * i] - mb1) / den1);
#pragma unroll
      for (int k = 0; k < 4; ++k) {
        a[2 * k] = fmaf(x0, lo16(g[i][k]), a[2 * k]); a[2 * k + 1] = fmaf(x0, hi16(g[i][k]), a[2 * k + 1]);
        a[8 + 2 * k] = fmaf(x0, lo16(u[i][k]), a[8 + 2 * k]); a[8 + 2 * k + 1] = fmaf(x0, hi16(u[i][k]), a[8 + 2 * k + 1]);
        a[16 + 2 * k] = fmaf(x1, lo16(g[i][k]), a[16 + 2 * k]); a[16 + 2 * k + 1] = fmaf(x1, hi16(g[i][k]), a[16 + 2 * k + 1]);
        a[24 + 2 * k] = fmaf(x1, lo16(u[i][k]), a[24 + 2 * k]); a[24 + 2 * k + 1] = fmaf(x1, hi16(u[i][k]), a[24 + 2 * k + 1]);
      }
    }
    TSTEP(16, lane & 16, x16)
    TSTEP(8, lane & 8, dpp<XOR8>)
    TSTEP(4, lane & 4, dpp<XOR7>)
    TSTEP(2, lane & 2, dpp<XOR2>)
    TSTEP(1, lane & 1, dpp<XOR1>)
    red[wave * 64 + lane] = a[0];
    __syncthreads();
    if (tid < 16) {
      const int cc = (tid >> 3) * 32 + (tid & 7);
      float g0v = 0.f, u0 = 0.f, g1v = 0.f, u1 = 0.f;
#pragma unroll
      for (int w2 = 0; w2 < 4; ++w2) { g0v += red[w2 * 64 + cc]; u0 += red[w2 * 64 + cc + 8]; g1v += red[w2 * 64 + cc + 16]; u1 += red[w2 * 64 + cc + 24]; }
      g0v = rbf(g0v); u0 = rbf(u0); g1v = rbf(g1v); u1 = rbf(u1);
      const unsigned short a0 = f2bf(rbf(g0v / (1.0f + __expf(-g0v))) * u0), a1 = f2bf(rbf(g1v / (1.0f + __expf(-g1v))) * u1);
      if (pass) __hip_atomic_store(g2 + j * 16 + tid, (1ull << 32) | ((unsigned)a1 << 16) | (unsigned)a0, RLX_AGENT);
    }
    __syncthreads();
    }
    STAMP(1, 3);
  }
}

extern "C" void kernel_launch(void* const* d_in, const int* in_sizes, int n_in,
                              void* d_out, int out_size, void* d_ws, size_t ws_size,
                              hipStream_t stream) {
  const unsigned short* x = (const unsigned short*)d_in[0];
  const unsigned short* kc = (const unsigned short*)d_in[1];
  const unsigned short* vc = (const unsigned short*)d_in[2];
  const unsigned short* wq = (const unsigned short*)d_in[3];
  const unsigned short* wo = (const unsigned short*)d_in[4];
  const unsigned short* wg = (const unsigned short*)d_in[6];
  const unsigned short* wu = (const unsigned short*)d_in[7];
  const unsigned short* wd = (const unsigned short*)d_in[8];
  unsigned char* ws = (unsigned char*)d_ws;
  k_attn<<<256, NTHR, 0, stream>>>(x, kc, vc, wq, ws);
  k_tail<<<TAIL_BLOCKS, 256, 0, stream>>>(x, wo, wg, wu, wd, ws, (unsigned short*)d_out);
}
```

```cpp
#define PRE 1
#define LBAR 1
#define DEP 4
#define PASS0 1
#define S2_DELAY 0
#include <hip/hip_runtime.h>
#include <hip/hip_bf16.h>
#include <stdint.h>

typedef unsigned u32x4 __attribute__((ext_vector_type(4)));
typedef float f32x4 __attribute__((ext_vector_type(4)));
typedef __bf16 bf16x2 __attribute__((ext_vector_type(2)));
typedef unsigned long long u64;

constexpr int NB = 2, DM = 512, NH = 32, SK = 16384, DH = 128, FF = 1792;
constexpr int NSPLIT = 4;
constexpr int KPS = SK / NSPLIT;
constexpr int NWAVE = 8, NTHR = 512;
constexpr int PAGE = 16;
constexpr int NPAGE = KPS / NWAVE / PAGE;
constexpr int NPART = NB * NH * NSPLIT;

__device__ __forceinline__ float bf2f(unsigned short b) { return __builtin_bit_cast(float, (unsigned)b << 16); }
__device__ __forceinline__ float rbf(float x) { return __bfloat162float(__float2bfloat16(x)); }
__device__ __forceinline__ unsigned short f2bf(float x) { return __builtin_bit_cast(unsigned short, __float2bfloat16(x)); }
__device__ __forceinline__ float lo16(unsigned u) { return __builtin_bit_cast(float, u << 16); }
__device__ __forceinline__ float hi16(unsigned u) { return __builtin_bit_cast(float, u & 0xffff0000u); }
__device__ __forceinline__ float dot2(unsigned k, unsigned q, float acc) {
  return __builtin_amdgcn_fdot2_f32_bf16(__builtin_bit_cast(bf16x2, k), __builtin_bit_cast(bf16x2, q), acc, false);
}
template <int CTRL> __device__ __forceinline__ float dpp(float x) {
  return __builtin_bit_cast(float, __builtin_amdgcn_mov_dpp(__builtin_bit_cast(int, x), CTRL, 0xf, 0xf, true));
}
constexpr int XOR1 = 0xB1, XOR2 = 0x4E, XOR7 = 0x141, XOR8 = 0x128, ROR4 = 0x124;
__device__ __forceinline__ float row16_sum(float x) {
  x += dpp<XOR1>(x); x += dpp<XOR2>(x); x += dpp<XOR7>(x); x += dpp<XOR8>(x); return x;
}
__device__ __forceinline__ float xrow_sum(float x) {
  auto s = __builtin_amdgcn_permlane16_swap(__float_as_uint(x), __float_as_uint(x), false, false);
  x = __uint_as_float(s[0]) + __uint_as_float(s[1]);
  auto t = __builtin_amdgcn_permlane32_swap(__float_as_uint(x), __float_as_uint(x), false, false);
  return __uint_as_float(t[0]) + __uint_as_float(t[1]);
}
__device__ __forceinline__ float xrow_max(float x) {
  auto s = __builtin_amdgcn_permlane16_swap(__float_as_uint(x), __float_as_uint(x), false, false);
  x = fmaxf(__uint_as_float(s[0]), __uint_as_float(s[1]));
  auto t = __builtin_amdgcn_permlane32_swap(__float_as_uint(x), __float_as_uint(x), false, false);
  return fmaxf(__uint_as_float(t[0]), __uint_as_float(t[1]));
}
__device__ __forceinline__ float wave_sum(float x) { return xrow_sum(row16_sum(x)); }

#define STAMP(k, i)
constexpr size_t WS_G0 = 0;
constexpr size_t WS_G1 = 262144;
constexpr size_t WS_G2 = WS_G1 + 4096;
constexpr size_t WS_GEND = WS_G2 + FF * 8;
constexpr size_t WS_Q = 512 * 1024;
constexpr size_t WS_PML = WS_Q + 16384;
constexpr size_t WS_PO = WS_PML + 2048;
constexpr size_t WS_GU = 1u << 20;
constexpr size_t WS_DT = 5u << 20;

__device__ __forceinline__ void ln8(const u32x4 xr, float (&v)[8]) {
#pragma unroll
  for (int k = 0; k < 4; ++k) { v[2 * k] = lo16(xr[k]); v[2 * k + 1] = hi16(xr[k]); }
  float s = ((v[0] + v[1]) + (v[2] + v[3])) + ((v[4] + v[5]) + (v[6] + v[7]));
  s = wave_sum(s);
  const float mean = s * (1.0f / 512.0f);
  float ss = 0.f;
#pragma unroll
  for (int i = 0; i < 8; ++i) { const float d = v[i] - mean; ss += d * d; }
  ss = wave_sum(ss);
  const float var = rbf(ss * (1.0f / 512.0f)), mb = rbf(mean);
  const float den = rbf(sqrtf(rbf(var + 1e-5f)));
#pragma unroll
  for (int i = 0; i < 8; ++i) v[i] = rbf(rbf(v[i] - mb) / den);
}

__global__ void __launch_bounds__(NTHR, 2) k_attn(const unsigned short* __restrict__ x, const unsigned short* __restrict__ kc,
                                                  const unsigned short* __restrict__ vc, const unsigned short* __restrict__ wq,
                                                  unsigned char* __restrict__ ws) {
  __shared__ __attribute__((aligned(16))) unsigned char smem[16384 + 4096 + NWAVE * 132 * 4];
  float* red = (float*)(smem + 16384);
  float* red2 = (float*)(smem + 16384 + 4096);
  const int tid = threadIdx.x, lane = tid & 63, wave = __builtin_amdgcn_readfirstlane(tid >> 6), r = lane >> 4, c = lane & 15;
  const int bid = blockIdx.x;
  const int b = bid >> 7, h = (bid >> 2) & 31, sp = bid & 3;
  STAMP(0, 0);
  const u32x4 xr = *(const u32x4*)(x + b * DM + lane * 8);
  const int rg = tid >> 4;
  u32x4 w[16];
  {
    const u32x4* wp = (const u32x4*)(wq + (size_t)h * DH) + c;
#pragma unroll
    for (int i = 0; i < 16; ++i) w[i] = __builtin_nontemporal_load(wp + (size_t)(rg + 32 * i) * (NH * DH / 8));
  }
  __builtin_amdgcn_sched_barrier(0);
  const size_t base = ((size_t)(b * NH + h) * SK + (size_t)sp * KPS) * DH;
  const u32x4* kp = (const u32x4*)(kc + base) + wave * 256 + r * 16 + c;
  const u32x4* vp = (const u32x4*)(vc + base) + wave * 256 + r * 16 + c;
  constexpr int DEPTH = DEP;
  u32x4 Kb[DEPTH][4], Vb[DEPTH][4];
#pragma unroll
  for (int d = 0; d < PRE; ++d)
#pragma unroll
    for (int t = 0; t < 4; ++t) {
      Kb[d][t] = __builtin_nontemporal_load(kp + d * 2048 + t * 64);
      Vb[d][t] = __builtin_nontemporal_load(vp + d * 2048 + t * 64);
    }
  __builtin_amdgcn_sched_barrier(0);
  float* lnw = (float*)smem + wave * 512;
  {
    float v[8];
    ln8(xr, v);
    *(f32x4*)(lnw + lane * 8) = f32x4{v[0], v[1], v[2], v[3]};
    *(f32x4*)(lnw + lane * 8 + 4) = f32x4{v[4], v[5], v[6], v[7]};
  }
  asm volatile("s_waitcnt lgkmcnt(0)" ::: "memory");
  {
    float a[8];
#pragma unroll
    for (int k = 0; k < 8; ++k) a[k] = 0.f;
#pragma unroll
    for (int i = 0; i < 16; ++i) {
      const float xl = lnw[rg + 32 * i];
#pragma unroll
      for (int k = 0; k < 4; ++k) { a[2 * k] = fmaf(xl, lo16(w[i][k]), a[2 * k]); a[2 * k + 1] = fmaf(xl, hi16(w[i][k]), a[2 * k + 1]); }
    }
#pragma unroll
    for (int k = 0; k < 8; ++k) a[k] = xrow_sum(a[k]);
    if (r == 0) {
      *(f32x4*)(red + wave * 128 + c * 8) = f32x4{a[0], a[1], a[2], a[3]};
      *(f32x4*)(red + wave * 128 + c * 8 + 4) = f32x4{a[4], a[5], a[6], a[7]};
    }
  }
  __syncthreads();
  u32x4 q;
  {
    f32x4 s0 = {0, 0, 0, 0}, s1 = {0, 0, 0, 0};
#pragma unroll
    for (int g = 0; g < 8; ++g) { s0 += *(const f32x4*)(red + g * 128 + c * 8); s1 += *(const f32x4*)(red + g * 128 + c * 8 + 4); }
    q[0] = (unsigned)f2bf(s0[0]) | ((unsigned)f2bf(s0[1]) << 16); q[1] = (unsigned)f2bf(s0[2]) | ((unsigned)f2bf(s0[3]) << 16);
    q[2] = (unsigned)f2bf(s1[0]) | ((unsigned)f2bf(s1[1]) << 16); q[3] = (unsigned)f2bf(s1[2]) | ((unsigned)f2bf(s1[3]) << 16);
  }
  STAMP(0, 1);
#pragma unroll
  for (int d = PRE; d < DEPTH; ++d)
#pragma unroll
    for (int t = 0; t < 4; ++t) {
      Kb[d][t] = __builtin_nontemporal_load(kp + d * 2048 + t * 64);
      Vb[d][t] = __builtin_nontemporal_load(vp + d * 2048 + t * 64);
    }
  __builtin_amdgcn_sched_barrier(0);
  {
    const int i = bid * 512 + tid;
    if (i < (int)(WS_GEND / 16)) ((u32x4*)ws)[i] = u32x4{0u, 0u, 0u, 0u};
  }
  __builtin_amdgcn_sched_barrier(0);
  STAMP(0, 5);
  constexpr float SCALE = 0.08838834764831845f, L2E = 1.4426950408889634f;
  float m = -INFINITY, l = 0.f, acc[8];
#pragma unroll
  for (int k = 0; k < 8; ++k) acc[k] = 0.f;
  constexpr int NIT = NPAGE / DEPTH;
  for (int it = 0; it < NIT; ++it) {
    const bool more = it + 1 < NIT;
    const int off = more ? (it + 1) * DEPTH * 2048 : -(wave * 256 + r * 16 + c);
    const int sd = more ? 2048 : 0, st = more ? 64 : 0;
#pragma unroll
    for (int d = 0; d < DEPTH; ++d) {
      float s[4];
#pragma unroll
      for (int t = 0; t < 4; ++t) {
        float dd = 0.f;
#pragma unroll
        for (int k = 0; k < 4; ++k) dd = dot2(Kb[d][t][k], q[k], dd);
        dd = row16_sum(dd);
        s[t] = rbf(rbf(dd) * SCALE) * L2E;
      }
      const float pm = fmaxf(fmaxf(s[0], s[1]), fmaxf(s[2], s[3]));
      const float mn = fmaxf(m, pm);
      const float alpha = __builtin_amdgcn_exp2f(m - mn);
      m = mn;
      float pp[4];
#pragma unroll
      for (int t = 0; t < 4; ++t) pp[t] = __builtin_amdgcn_exp2f(s[t] - mn);
      l = fmaf(l, alpha, (pp[0] + pp[1]) + (pp[2] + pp[3]));
#pragma unroll
      for (int k = 0; k < 8; ++k) acc[k] *= alpha;
#pragma unroll
      for (int t = 0; t < 4; ++t)
#pragma unroll
        for (int k = 0; k < 4; ++k) {
          acc[2 * k] = fmaf(pp[t], lo16(Vb[d][t][k]), acc[2 * k]);
          acc[2 * k + 1] = fmaf(pp[t], hi16(Vb[d][t][k]), acc[2 * k + 1]);
        }
      asm volatile("" : "+v"(acc[0]), "+v"(acc[1]), "+v"(acc[2]), "+v"(acc[3]), "+v"(acc[4]), "+v"(acc[5]), "+v"(acc[6]), "+v"(acc[7]));
      __builtin_amdgcn_sched_barrier(0);
#pragma unroll
      for (int t = 0; t < 4; ++t) {
        Kb[d][t] = __builtin_nontemporal_load(kp + off + d * sd + t * st);
        Vb[d][t] = __builtin_nontemporal_load(vp + off + d * sd + t * st);
      }
      __builtin_amdgcn_sched_barrier(0);
    }
    __builtin_amdgcn_s_barrier();
  }
  STAMP(0, 2);
  {
    const float M = xrow_max(m);
    const float wgt = __builtin_amdgcn_exp2f(m - M);
    const float L = xrow_sum(l * wgt);
#pragma unroll
    for (int k = 0; k < 8; ++k) acc[k] = xrow_sum(acc[k] * wgt);
    float* rec = red2 + wave * 132;
    if (r == 0) {
      *(f32x4*)(rec + 4 + 8 * c) = f32x4{acc[0], acc[1], acc[2], acc[3]};
      *(f32x4*)(rec + 8 + 8 * c) = f32x4{acc[4], acc[5], acc[6], acc[7]};
      if (c == 0) { rec[0] = M; rec[1] = L; }
    }
  }
  __syncthreads();
  if (tid < DH) {
    float M = -INFINITY;
#pragma unroll
    for (int g = 0; g < NWAVE; ++g) M = fmaxf(M, red2[g * 132]);
    float L = 0.f, o = 0.f;
#pragma unroll
    for (int g = 0; g < NWAVE; ++g) {
      const float wgt = __builtin_amdgcn_exp2f(red2[g * 132] - M);
      L = fmaf(wgt, red2[g * 132 + 1], L);
      o = fmaf(wgt, red2[g * 132 + 4 + tid], o);
    }
    ((float*)(ws + WS_PO))[(size_t)bid * DH + tid] = o;
    if (tid == 0) ((float2*)(ws + WS_PML))[bid] = make_float2(M, L);
  }
  STAMP(0, 3);
}

#define RLX_AGENT __ATOMIC_RELAXED, __HIP_MEMORY_SCOPE_AGENT
constexpr unsigned SPIN_CAP = 1u << 22;

constexpr int TAIL_BLOCKS = 128 + 112;
#define TSTEP(N, BIT, XCHG)                                                                       \
  _Pragma("unroll") for (int i_ = 0; i_ < (N); ++i_) {                                            \
    const float keep_ = (BIT) ? a[i_ + (N)] : a[i_], send_ = (BIT) ? a[i_] : a[i_ + (N)];         \
    a[i_] = keep_ + XCHG(send_);                                                                  \
  }
__device__ __forceinline__ float x16(float v) {
  auto s = __builtin_amdgcn_permlane16_swap(__float_as_uint(v), __float_as_uint(v), false, false);
  return __uint_as_float((__lane_id() & 16) ? s[0] : s[1]);
}
__device__ __forceinline__ float x32(float v) {
  auto s = __builtin_amdgcn_permlane32_swap(__float_as_uint(v), __float_as_uint(v), false, false);
  return __uint_as_float((__lane_id() & 32) ? s[0] : s[1]);
}
__global__ void __launch_bounds__(256) k_tail(const unsigned short* __restrict__ x, const unsigned short* __restrict__ wo,
                                              const unsigned short* __restrict__ wg, const unsigned short* __restrict__ wu,
                                              const unsigned short* __restrict__ wd, unsigned char* ws, unsigned short* __restrict__ out) {
  __shared__ __attribute__((aligned(16))) float hs[NB * DM];
  __shared__ __attribute__((aligned(16))) float red[4 * 256];
  __shared__ __attribute__((aligned(16))) float os[NB * DH];
  const int tid = threadIdx.x, lane = tid & 63, wave = tid >> 6, bid = blockIdx.x;
  u64* g0 = (u64*)(ws + WS_G0);
  u64* g1 = (u64*)(ws + WS_G1);
  u64* g2 = (u64*)(ws + WS_G2);
  if (bid < 128) {
    const int j = bid;
    STAMP(1, 0);
    const int h = j >> 2, cq = j & 3;
    const int sb = tid >> 7, sd = tid & 127;
    const int p0 = (sb * NH + h) * NSPLIT;
    const f32x4 ml0 = *(const f32x4*)(ws + WS_PML + (size_t)p0 * 8), ml1 = *(const f32x4*)(ws + WS_PML + (size_t)p0 * 8 + 16);
    float po[4];
#pragma unroll
    for (int s = 0; s < 4; ++s) po[s] = ((const float*)(ws + WS_PO))[(size_t)(p0 + s) * DH + sd];
    const int cg = tid & 15, rg0 = tid >> 4;
    u32x4 wv[8];
#pragma unroll
    for (int i = 0; i < 8; ++i) wv[i] = __builtin_nontemporal_load((const u32x4*)(wo + (size_t)(h * DH + rg0 + 16 * i) * DM + cq * 128 + cg * 8));
    const unsigned short xr = x[((tid >> 3) & 1) * DM + (j & 63) * 8 + (tid & 7)];
    __builtin_amdgcn_sched_barrier(0);
    {
      const float M = fmaxf(fmaxf(ml0[0], ml0[2]), fmaxf(ml1[0], ml1[2]));
      const float w0 = __builtin_amdgcn_exp2f(ml0[0] - M), w1 = __builtin_amdgcn_exp2f(ml0[2] - M);
      const float w2 = __builtin_amdgcn_exp2f(ml1[0] - M), w3 = __builtin_amdgcn_exp2f(ml1[2] - M);
      const float L = fmaf(w3, ml1[3], fmaf(w2, ml1[1], fmaf(w1, ml0[3], w0 * ml0[1])));
      const float o = fmaf(w3, po[3], fmaf(w2, po[2], fmaf(w1, po[1], w0 * po[0])));
      os[tid] = rbf(o / L);
    }
    __syncthreads();
    {
      float a[16];
#pragma unroll
      for (int k = 0; k < 16; ++k) a[k] = 0.f;
#pragma unroll
      for (int i = 0; i < 8; ++i) {
        const float o0 = os[rg0 + 16 * i], o1 = os[DH + rg0 + 16 * i];
#pragma unroll
        for (int k = 0; k < 4; ++k) {
          a[2 * k] = fmaf(o0, lo16(wv[i][k]), a[2 * k]); a[2 * k + 1] = fmaf(o0, hi16(wv[i][k]), a[2 * k + 1]);
          a[8 + 2 * k] = fmaf(o1, lo16(wv[i][k]), a[8 + 2 * k]); a[8 + 2 * k + 1] = fmaf(o1, hi16(wv[i][k]), a[8 + 2 * k + 1]);
        }
      }
      TSTEP(8, lane & 32, x32)
      TSTEP(4, lane & 16, x16)
      *(f32x4*)(red + wave * 256 + (lane >> 5) * 128 + cg * 8 + ((lane >> 4) & 1) * 4) = f32x4{a[0], a[1], a[2], a[3]};
    }
    __syncthreads();
    {
      const float y = (red[tid] + red[256 + tid]) + (red[512 + tid] + red[768 + tid]);
      const int col = cq * 128 + sd;
      __hip_atomic_store(g0 + (((size_t)(col >> 3) * NH + h) * 2 + sb) * 8 + (col & 7), (1ull << 32) | (u64)__float_as_uint(y), RLX_AGENT);
    }
    STAMP(1, 6);
    if (j < 64) {
#pragma unroll 1
      for (int pass = PASS0; pass < 2; ++pass) {
      asm volatile("" : "+s"(pass));
      u64 a0, a1; unsigned spins = 0;
      do {
        a0 = __hip_atomic_load(g0 + (size_t)j * 512 + tid, RLX_AGENT);
        a1 = __hip_atomic_load(g0 + (size_t)j * 512 + 256 + tid, RLX_AGENT);
      } while (pass && (((a0 & a1) >> 32) != 1ull) && ++spins < SPIN_CAP);
      float s = __uint_as_float((unsigned)a0) + __uint_as_float((unsigned)a1);
      s = xrow_sum(s);
      if (lane < 16) os[wave * 16 + lane] = s;
      __syncthreads();
      if (tid < 16) {
        const float t = (os[tid] + os[16 + tid]) + (os[32 + tid] + os[48 + tid]);
        const float hv = rbf(bf2f(xr) + rbf(t));
        const float other = __builtin_bit_cast(float, __builtin_amdgcn_mov_dpp(__builtin_bit_cast(int, hv), XOR8, 0xf, 0xf, true));
        if (pass && tid < 8) __hip_atomic_store(g1 + j * 8 + tid, (1ull << 32) | (__float_as_uint(other) & 0xffff0000u) | (__float_as_uint(hv) >> 16), RLX_AGENT);
      }
      __syncthreads();
      }
      STAMP(1, 1);
    } else {
      const int e = j - 64, jj = (e & 7) * 8 + (e >> 3);
      u32x4 d[7];
#pragma unroll
      for (int i = 0; i < 7; ++i) d[i] = __builtin_nontemporal_load((const u32x4*)(wd + (size_t)(tid + 256 * i) * DM + jj * 8));
      __builtin_amdgcn_sched_barrier(0);
#pragma unroll 1
      for (int pass = PASS0; pass < 2; ++pass) {
      asm volatile("" : "+s"(pass));
      u64 av[7], hv; unsigned spins = 0; bool ok;
      do {
        ok = true;
#pragma unroll
        for (int i = 0; i < 7; ++i) { av[i] = __hip_atomic_load(g2 + tid + 256 * i, RLX_AGENT); }
        hv = __hip_atomic_load(g1 + jj * 8 + (tid & 7), RLX_AGENT);
#pragma unroll
        for (int i = 0; i < 7; ++i) ok &= (av[i] >> 32) == 1ull;
        ok &= (hv >> 32) == 1ull;
      } while (pass && !ok && ++spins < SPIN_CAP);
      float a[16];
#pragma unroll
      for (int k = 0; k < 16; ++k) a[k] = 0.f;
#pragma unroll
      for (int i = 0; i < 7; ++i) {
        const float a0 = lo16((unsigned)av[i]), a1 = hi16((unsigned)av[i]);
#pragma unroll
        for (int k = 0; k < 4; ++k) {
          a[2 * k] = fmaf(a0, lo16(d[i][k]), a[2 * k]); a[2 * k + 1] = fmaf(a0, hi16(d[i][k]), a[2 * k + 1]);
          a[8 + 2 * k] = fmaf(a1, lo16(d[i][k]), a[8 + 2 * k]); a[8 + 2 * k + 1] = fmaf(a1, hi16(d[i][k]), a[8 + 2 * k + 1]);
        }
      }
      TSTEP(8, lane & 32, x32)
      TSTEP(4, lane & 16, x16)
      TSTEP(2, lane & 8, dpp<XOR8>)
      TSTEP(1, lane & 1, dpp<XOR1>)
      float t = a[0];
      t += dpp<XOR2>(t);
      t += dpp<XOR1>(dpp<XOR7>(t));
      if ((lane & 6) == 0) red[wave * 16 + ((lane >> 5) & 1) * 8 + ((lane >> 4) & 1) * 4 + ((lane >> 3) & 1) * 2 + (lane & 1)] = t;
      __syncthreads();
      if (pass && tid < 16) {
        const float s = (red[tid] + red[16 + tid]) + (red[32 + tid] + red[48 + tid]);
        const float hres = (tid >> 3) ? hi16((unsigned)hv) : lo16((unsigned)hv);
        out[(tid >> 3) * DM + jj * 8 + (tid & 7)] = f2bf(hres + rbf(s));
      }
      __syncthreads();
      }
    }
  } else {
    const int e = bid - 128, j = e < 96 ? ((e >> 5) * 8 + (e & 7)) * 4 + ((e >> 3) & 3) : (24 + ((e - 96) & 3)) * 4 + ((e - 96) >> 2);
    const int ch = lane >> 5, rg = wave * 32 + (lane & 31);
    u32x4 g[4], u[4];
#pragma unroll
    for (int i = 0; i < 4; ++i) {
      g[i] = __builtin_nontemporal_load((const u32x4*)(wg + (size_t)(rg + 128 * i) * FF + j * 16 + ch * 8));
      u[i] = __builtin_nontemporal_load((const u32x4*)(wu + (size_t)(rg + 128 * i) * FF + j * 16 + ch * 8));
    }
    __builtin_amdgcn_sched_barrier(0);
#pragma unroll 1
    for (int pass = PASS0; pass < 2; ++pass) {
    asm volatile("" : "+s"(pass));
    {
      u64 a0, a1; unsigned spins = 0;
      do {
        a0 = __hip_atomic_load(g1 + tid, RLX_AGENT);
        a1 = __hip_atomic_load(g1 + 256 + tid, RLX_AGENT);
      } while (pass && (((a0 & a1) >> 32) != 1ull) && ++spins < SPIN_CAP);
      hs[tid] = lo16((unsigned)a0); hs[DM + tid] = hi16((unsigned)a0);
      hs[256 + tid] = lo16((unsigned)a1); hs[DM + 256 + tid] = hi16((unsigned)a1);
    }
    __syncthreads();
    if (pass) { STAMP(1, 2); }
    float mb0, mb1, den0, den1;
    {
      const f32x4 p0 = *(const f32x4*)(hs + lane * 8), p1 = *(const f32x4*)(hs + lane * 8 + 4);
      const f32x4 r0 = *(const f32x4*)(hs + DM + lane * 8), r1 = *(const f32x4*)(hs + DM + lane * 8 + 4);
      float s0 = ((p0[0] + p0[1]) + (p0[2] + p0[3])) + ((p1[0] + p1[1]) + (p1[2] + p1[3]));
      float s1 = ((r0[0] + r0[1]) + (r0[2] + r0[3])) + ((r1[0] + r1[1]) + (r1[2] + r1[3]));
      s0 = wave_sum(s0); s1 = wave_sum(s1);
      const float mean0 = s0 * (1.0f / 512.0f), mean1 = s1 * (1.0f / 512.0f);
      float q0 = 0.f, q1 = 0.f;
#pragma unroll
      for (int i = 0; i < 4; ++i) {
        float d0 = p0[i] - mean0, d1 = r0[i] - mean1; q0 += d0 * d0; q1 += d1 * d1;
        d0 = p1[i] - mean0; d1 = r1[i] - mean1; q0 += d0 * d0; q1 += d1 * d1;
      }
      q0 = wave_sum(q0); q1 = wave_sum(q1);
      mb0 = rbf(mean0); mb1 = rbf(mean1);
      den0 = rbf(sqrtf(rbf(rbf(q0 * (1.0f / 512.0f)) + 1e-5f)));
      den1 = rbf(sqrtf(rbf(rbf(q1 * (1.0f / 512.0f)) + 1e-5f)));
    }
    float a[32];
#pragma unroll
    for (int k = 0; k < 32; ++k) a[k] = 0.f;
#pragma unroll
    for (int i = 0; i < 4; ++i) {
      const float x0 = rbf(rbf(hs[rg + 128 * i] - mb0) / den0), x1 = rbf(rbf(hs[DM + rg + 128 * i] - mb1) / den1);
#pragma unroll
      for (int k = 0; k < 4; ++k) {
        a[2 * k] = fmaf(x0, lo16(g[i][k]), a[2 * k]); a[2 * k + 1] = fmaf(x0, hi16(g[i][k]), a[2 * k + 1]);
        a[8 + 2 * k] = fmaf(x0, lo16(u[i][k]), a[8 + 2 * k]); a[8 + 2 * k + 1] = fmaf(x0, hi16(u[i][k]), a[8 + 2 * k + 1]);
        a[16 + 2 * k] = fmaf(x1, lo16(g[i][k]), a[16 + 2 * k]); a[16 + 2 * k + 1] = fmaf(x1, hi16(g[i][k]), a[16 + 2 * k + 1]);
        a[24 + 2 * k] = fmaf(x1, lo16(u[i][k]), a[24 + 2 * k]); a[24 + 2 * k + 1] = fmaf(x1, hi16(u[i][k]), a[24 + 2 * k + 1]);
      }
    }
    TSTEP(16, lane & 16, x16)
    TSTEP(8, lane & 8, dpp<XOR8>)
    TSTEP(4, lane & 4, dpp<XOR7>)
    TSTEP(2, lane & 2, dpp<XOR2>)
    TSTEP(1, lane & 1, dpp<XOR1>)
    red[wave * 64 + lane] = a[0];
    __syncthreads();
    if (tid < 16) {
      const int cc = (tid >> 3) * 32 + (tid & 7);
      float g0v = 0.f, u0 = 0.f, g1v = 0.f, u1 = 0.f;
#pragma unroll
      for (int w2 = 0; w2 < 4; ++w2) { g0v += red[w2 * 64 + cc]; u0 += red[w2 * 64 + cc + 8]; g1v += red[w2 * 64 + cc + 16]; u1 += red[w2 * 64 + cc + 24]; }
      g0v = rbf(g0v); u0 = rbf(u0); g1v = rbf(g1v); u1 = rbf(u1);
      const unsigned short a0 = f2bf(rbf(g0v / (1.0f + __expf(-g0v))) * u0), a1 = f2bf(rbf(g1v / (1.0f + __expf(-g1v))) * u1);
      if (pass) __hip_atomic_store(g2 + j * 16 + tid, (1ull << 32) | ((unsigned)a1 << 16) | (unsigned)a0, RLX_AGENT);
    }
    __syncthreads();
    }
    STAMP(1, 3);
  }
}

extern "C" void kernel_launch(void* const* d_in, const int* in_sizes, int n_in,
                              void* d_out, int out_size, void* d_ws, size_t ws_size,
                              hipStream_t stream) {
  const unsigned short* x = (const unsigned short*)d_in[0];
  const unsigned short* kc = (const unsigned short*)d_in[1];
  const unsigned short* vc = (const unsigned short*)d_in[2];
  const unsigned short* wq = (const unsigned short*)d_in[3];
  const unsigned short* wo = (const unsigned short*)d_in[4];
  const unsigned short* wg = (const unsigned short*)d_in[6];
  const unsigned short* wu = (const unsigned short*)d_in[7];
  const unsigned short* wd = (const unsigned short*)d_in[8];
  unsigned char* ws = (unsigned char*)d_ws;
  k_attn<<<256, NTHR, 0, stream>>>(x, kc, vc, wq, ws);
  k_tail<<<TAIL_BLOCKS, 256, 0, stream>>>(x, wo, wg, wu, wd, ws, (unsigned short*)d_out);
}
```

```cpp
#define DEP 2
#define PRE 1
#define LBAR 1
#define PASS0 1
#define S2_DELAY 0
#include <hip/hip_runtime.h>
#include <hip/hip_bf16.h>
#include <stdint.h>

typedef unsigned u32x4 __attribute__((ext_vector_type(4)));
typedef float f32x4 __attribute__((ext_vector_type(4)));
typedef __bf16 bf16x2 __attribute__((ext_vector_type(2)));
typedef unsigned long long u64;

constexpr int NB = 2, DM = 512, NH = 32, SK = 16384, DH = 128, FF = 1792;
constexpr int NSPLIT = 4;
constexpr int KPS = SK / NSPLIT;
constexpr int NWAVE = 8, NTHR = 512;
constexpr int PAGE = 16;
constexpr int NPAGE = KPS / NWAVE / PAGE;
constexpr int NPART = NB * NH * NSPLIT;

__device__ __forceinline__ float bf2f(unsigned short b) { return __builtin_bit_cast(float, (unsigned)b << 16); }
__device__ __forceinline__ float rbf(float x) { return __bfloat162float(__float2bfloat16(x)); }
__device__ __forceinline__ unsigned short f2bf(float x) { return __builtin_bit_cast(unsigned short, __float2bfloat16(x)); }
__device__ __forceinline__ float lo16(unsigned u) { return __builtin_bit_cast(float, u << 16); }
__device__ __forceinline__ float hi16(unsigned u) { return __builtin_bit_cast(float, u & 0xffff0000u); }
__device__ __forceinline__ float dot2(unsigned k, unsigned q, float acc) {
  return __builtin_amdgcn_fdot2_f32_bf16(__builtin_bit_cast(bf16x2, k), __builtin_bit_cast(bf16x2, q), acc, false);
}
template <int CTRL> __device__ __forceinline__ float dpp(float x) {
  return __builtin_bit_cast(float, __builtin_amdgcn_mov_dpp(__builtin_bit_cast(int, x), CTRL, 0xf, 0xf, true));
}
constexpr int XOR1 = 0xB1, XOR2 = 0x4E, XOR7 = 0x141, XOR8 = 0x128, ROR4 = 0x124;
__device__ __forceinline__ float row16_sum(float x) {
  x += dpp<XOR1>(x); x += dpp<XOR2>(x); x += dpp<XOR7>(x); x += dpp<XOR8>(x); return x;
}
__device__ __forceinline__ float xrow_sum(float x) {
  auto s = __builtin_amdgcn_permlane16_swap(__float_as_uint(x), __float_as_uint(x), false, false);
  x = __uint_as_float(s[0]) + __uint_as_float(s[1]);
  auto t = __builtin_amdgcn_permlane32_swap(__float_as_uint(x), __float_as_uint(x), false, false);
  return __uint_as_float(t[0]) + __uint_as_float(t[1]);
}
__device__ __forceinline__ float xrow_max(float x) {
  auto s = __builtin_amdgcn_permlane16_swap(__float_as_uint(x), __float_as_uint(x), false, false);
  x = fmaxf(__uint_as_float(s[0]), __uint_as_float(s[1]));
  auto t = __builtin_amdgcn_permlane32_swap(__float_as_uint(x), __float_as_uint(x), false, false);
  return fmaxf(__uint_as_float(t[0]), __uint_as_float(t[1]));
}
__device__ __forceinline__ float wave_sum(float x) { return xrow_sum(row16_sum(x)); }

#define STAMP(k, i)
constexpr size_t WS_G0 = 0;
constexpr size_t WS_G1 = 262144;
constexpr size_t WS_G2 = WS_G1 + 4096;
constexpr size_t WS_GEND = WS_G2 + FF * 8;
constexpr size_t WS_Q = 512 * 1024;
constexpr size_t WS_PML = WS_Q + 16384;
constexpr size_t WS_PO = WS_PML + 2048;
constexpr size_t WS_GU = 1u << 20;
constexpr size_t WS_DT = 5u << 20;

__device__ __forceinline__ void ln8(const u32x4 xr, float (&v)[8]) {
#pragma unroll
  for (int k = 0; k < 4; ++k) { v[2 * k] = lo16(xr[k]); v[2 * k + 1] = hi16(xr[k]); }
  float s = ((v[0] + v[1]) + (v[2] + v[3])) + ((v[4] + v[5]) + (v[6] + v[7]));
  s = wave_sum(s);
  const float mean = s * (1.0f / 512.0f);
  float ss = 0.f;
#pragma unroll
  for (int i = 0; i < 8; ++i) { const float d = v[i] - mean; ss += d * d; }
  ss = wave_sum(ss);
  const float var = rbf(ss * (1.0f / 512.0f)), mb = rbf(mean);
  const float den = rbf(sqrtf(rbf(var + 1e-5f)));
#pragma unroll
  for (int i = 0; i < 8; ++i) v[i] = rbf(rbf(v[i] - mb) / den);
}

__global__ void __launch_bounds__(NTHR, 2) k_attn(const unsigned short* __restrict__ x, const unsigned short* __restrict__ kc,
                                                  const unsigned short* __restrict__ vc, const unsigned short* __restrict__ wq,
                                                  unsigned char* __restrict__ ws) {
  __shared__ __attribute__((aligned(16))) unsigned char smem[16384 + 4096 + NWAVE * 132 * 4];
  float* red = (float*)(smem + 16384);
  float* red2 = (float*)(smem + 16384 + 4096);
  const int tid = threadIdx.x, lane = tid & 63, wave = __builtin_amdgcn_readfirstlane(tid >> 6), r = lane >> 4, c = lane & 15;
  const int bid = blockIdx.x;
  const int b = bid >> 7, h = (bid >> 2) & 31, sp = bid & 3;
  STAMP(0, 0);
  const u32x4 xr = *(const u32x4*)(x + b * DM + lane * 8);
  const int rg = tid >> 4;
  u32x4 w[16];
  {
    const u32x4* wp = (const u32x4*)(wq + (size_t)h * DH) + c;
#pragma unroll
    for (int i = 0; i < 16; ++i) w[i] = __builtin_nontemporal_load(wp + (size_t)(rg + 32 * i) * (NH * DH / 8));
  }
  __builtin_amdgcn_sched_barrier(0);
  const size_t base = ((size_t)(b * NH + h) * SK + (size_t)sp * KPS) * DH;
  const u32x4* kp = (const u32x4*)(kc + base) + wave * 256 + r * 16 + c;
  const u32x4* vp = (const u32x4*)(vc + base) + wave * 256 + r * 16 + c;
  constexpr int DEPTH = DEP;
  u32x4 Kb[DEPTH][4], Vb[DEPTH][4];
#pragma unroll
  for (int d = 0; d < PRE; ++d)
#pragma unroll
    for (int t = 0; t < 4; ++t) {
      Kb[d][t] = __builtin_nontemporal_load(kp + d * 2048 + t * 64);
      Vb[d][t] = __builtin_nontemporal_load(vp + d * 2048 + t * 64);
    }
  __builtin_amdgcn_sched_barrier(0);
  float* lnw = (float*)smem + wave * 512;
  {
    float v[8];
    ln8(xr, v);
    *(f32x4*)(lnw + lane * 8) = f32x4{v[0], v[1], v[2], v[3]};
    *(f32x4*)(lnw + lane * 8 + 4) = f32x4{v[4], v[5], v[6], v[7]};
  }
  asm volatile("s_waitcnt lgkmcnt(0)" ::: "memory");
  {
    float a[8];
#pragma unroll
    for (int k = 0; k < 8; ++k) a[k] = 0.f;
#pragma unroll
    for (int i = 0; i < 16; ++i) {
      const float xl = lnw[rg + 32 * i];
#pragma unroll
      for (int k = 0; k < 4; ++k) { a[2 * k] = fmaf(xl, lo16(w[i][k]), a[2 * k]); a[2 * k + 1] = fmaf(xl, hi16(w[i][k]), a[2 * k + 1]); }
    }
#pragma unroll
    for (int k = 0; k < 8; ++k) a[k] = xrow_sum(a[k]);
    if (r == 0) {
      *(f32x4*)(red + wave * 128 + c * 8) = f32x4{a[0], a[1], a[2], a[3]};
      *(f32x4*)(red + wave * 128 + c * 8 + 4) = f32x4{a[4], a[5], a[6], a[7]};
    }
  }
  __syncthreads();
  u32x4 q;
  {
    f32x4 s0 = {0, 0, 0, 0}, s1 = {0, 0, 0, 0};
#pragma unroll
    for (int g = 0; g < 8; ++g) { s0 += *(const f32x4*)(red + g * 128 + c * 8); s1 += *(const f32x4*)(red + g * 128 + c * 8 + 4); }
    q[0] = (unsigned)f2bf(s0[0]) | ((unsigned)f2bf(s0[1]) << 16); q[1] = (unsigned)f2bf(s0[2]) | ((unsigned)f2bf(s0[3]) << 16);
    q[2] = (unsigned)f2bf(s1[0]) | ((unsigned)f2bf(s1[1]) << 16); q[3] = (unsigned)f2bf(s1[2]) | ((unsigned)f2bf(s1[3]) << 16);
  }
  STAMP(0, 1);
#pragma unroll
  for (int d = PRE; d < DEPTH; ++d)
#pragma unroll
    for (int t = 0; t < 4; ++t) {
      Kb[d][t] = __builtin_nontemporal_load(kp + d * 2048 + t * 64);
      Vb[d][t] = __builtin_nontemporal_load(vp + d * 2048 + t * 64);
    }
  __builtin_amdgcn_sched_barrier(0);
  {
    const int i = bid * 512 + tid;
    if (i < (int)(WS_GEND / 16)) ((u32x4*)ws)[i] = u32x4{0u, 0u, 0u, 0u};
  }
  __builtin_amdgcn_sched_barrier(0);
  STAMP(0, 5);
  constexpr float SCALE = 0.08838834764831845f, L2E = 1.4426950408889634f;
  float m = -INFINITY, l = 0.f, acc[8];
#pragma unroll
  for (int k = 0; k < 8; ++k) acc[k] = 0.f;
  constexpr int NIT = NPAGE / DEPTH;
  for (int it = 0; it < NIT; ++it) {
    const bool more = it + 1 < NIT;
    const int off = more ? (it + 1) * DEPTH * 2048 : -(wave * 256 + r * 16 + c);
    const int sd = more ? 2048 : 0, st = more ? 64 : 0;
#pragma unroll
    for (int d = 0; d < DEPTH; ++d) {
      float s[4];
#pragma unroll
      for (int t = 0; t < 4; ++t) {
        float dd = 0.f;
#pragma unroll
        for (int k = 0; k < 4; ++k) dd = dot2(Kb[d][t][k], q[k], dd);
        dd = row16_sum(dd);
        s[t] = rbf(rbf(dd) * SCALE) * L2E;
      }
      const float pm = fmaxf(fmaxf(s[0], s[1]), fmaxf(s[2], s[3]));
      const float mn = fmaxf(m, pm);
      const float alpha = __builtin_amdgcn_exp2f(m - mn);
      m = mn;
      float pp[4];
#pragma unroll
      for (int t = 0; t < 4; ++t) pp[t] = __builtin_amdgcn_exp2f(s[t] - mn);
      l = fmaf(l, alpha, (pp[0] + pp[1]) + (pp[2] + pp[3]));
#pragma unroll
      for (int k = 0; k < 8; ++k) acc[k] *= alpha;
#pragma unroll
      for (int t = 0; t < 4; ++t)
#pragma unroll
        for (int k = 0; k < 4; ++k) {
          acc[2 * k] = fmaf(pp[t], lo16(Vb[d][t][k]), acc[2 * k]);
          acc[2 * k + 1] = fmaf(pp[t], hi16(Vb[d][t][k]), acc[2 * k + 1]);
        }
      asm volatile("" : "+v"(acc[0]), "+v"(acc[1]), "+v"(acc[2]), "+v"(acc[3]), "+v"(acc[4]), "+v"(acc[5]), "+v"(acc[6]), "+v"(acc[7]));
      __builtin_amdgcn_sched_barrier(0);
#pragma unroll
      for (int t = 0; t < 4; ++t) {
        Kb[d][t] = __builtin_nontemporal_load(kp + off + d * sd + t * st);
        Vb[d][t] = __builtin_nontemporal_load(vp + off + d * sd + t * st);
      }
      __builtin_amdgcn_sched_barrier(0);
    }
    __builtin_amdgcn_s_barrier();
  }
  STAMP(0, 2);
  {
    const float M = xrow_max(m);
    const float wgt = __builtin_amdgcn_exp2f(m - M);
    const float L = xrow_sum(l * wgt);
#pragma unroll
    for (int k = 0; k < 8; ++k) acc[k] = xrow_sum(acc[k] * wgt);
    float* rec = red2 + wave * 132;
    if (r == 0) {
      *(f32x4*)(rec + 4 + 8 * c) = f32x4{acc[0], acc[1], acc[2], acc[3]};
      *(f32x4*)(rec + 8 + 8 * c) = f32x4{acc[4], acc[5], acc[6], acc[7]};
      if (c == 0) { rec[0] = M; rec[1] = L; }
    }
  }
  __syncthreads();
  if (tid < DH) {
    float M = -INFINITY;
#pragma unroll
    for (int g = 0; g < NWAVE; ++g) M = fmaxf(M, red2[g * 132]);
    float L = 0.f, o = 0.f;
#pragma unroll
    for (int g = 0; g < NWAVE; ++g) {
      const float wgt = __builtin_amdgcn_exp2f(red2[g * 132] - M);
      L = fmaf(wgt, red2[g * 132 + 1], L);
      o = fmaf(wgt, red2[g * 132 + 4 + tid], o);
    }
    ((float*)(ws + WS_PO))[(size_t)bid * DH + tid] = o;
    if (tid == 0) ((float2*)(ws + WS_PML))[bid] = make_float2(M, L);
  }
  STAMP(0, 3);
}

#define RLX_AGENT __ATOMIC_RELAXED, __HIP_MEMORY_SCOPE_AGENT
constexpr unsigned SPIN_CAP = 1u << 22;

constexpr int TAIL_BLOCKS = 128 + 112;
#define TSTEP(N, BIT, XCHG)                                                                       \
  _Pragma("unroll") for (int i_ = 0; i_ < (N); ++i_) {                                            \
    const float keep_ = (BIT) ? a[i_ + (N)] : a[i_], send_ = (BIT) ? a[i_] : a[i_ + (N)];         \
    a[i_] = keep_ + XCHG(send_);                                                                  \
  }
__device__ __forceinline__ float x16(float v) {
  auto s = __builtin_amdgcn_permlane16_swap(__float_as_uint(v), __float_as_uint(v), false, false);
  return __uint_as_float((__lane_id() & 16) ? s[0] : s[1]);
}
__device__ __forceinline__ float x32(float v) {
  auto s = __builtin_amdgcn_permlane32_swap(__float_as_uint(v), __float_as_uint(v), false, false);
  return __uint_as_float((__lane_id() & 32) ? s[0] : s[1]);
}
typedef short s16x4 __attribute__((ext_vector_type(4)));
typedef __bf16 bf16x8 __attribute__((ext_vector_type(8)));
typedef unsigned u32x2 __attribute__((ext_vector_type(2)));
#define LAS __attribute__((address_space(3)))
__device__ __forceinline__ u32x4 tr_frag(const unsigned char* p, int pitch4) {
  const u32x2 a = __builtin_bit_cast(u32x2, __builtin_amdgcn_ds_read_tr16_b64_v4i16((LAS s16x4*)p));
  const u32x2 b = __builtin_bit_cast(u32x2, __builtin_amdgcn_ds_read_tr16_b64_v4i16((LAS s16x4*)(p + pitch4)));
  return u32x4{a[0], a[1], b[0], b[1]};
}
__device__ __forceinline__ f32x4 mfma16(u32x4 a, u32x4 b, f32x4 c) {
  return __builtin_amdgcn_mfma_f32_16x16x32_bf16(__builtin_bit_cast(bf16x8, a), __builtin_bit_cast(bf16x8, b), c, 0, 0, 0);
}
__global__ void __launch_bounds__(256) k_tail(const unsigned short* __restrict__ x, const unsigned short* __restrict__ wo,
                                              const unsigned short* __restrict__ wg, const unsigned short* __restrict__ wu,
                                              const unsigned short* __restrict__ wd, unsigned char* ws, unsigned short* __restrict__ out) {
  __shared__ __attribute__((aligned(16))) float hs[NB * DM];
  __shared__ __attribute__((aligned(16))) float red[4 * 256];
  __shared__ __attribute__((aligned(16))) float os[NB * DH];
  __shared__ __attribute__((aligned(16))) unsigned char wst[32768 + 64];
  __shared__ __attribute__((aligned(16))) unsigned short abuf[NB * FF + 8];
  const int tid = threadIdx.x, lane = tid & 63, wave = tid >> 6, bid = blockIdx.x;
  u64* g0 = (u64*)(ws + WS_G0);
  u64* g1 = (u64*)(ws + WS_G1);
  u64* g2 = (u64*)(ws + WS_G2);
  if (bid < 128) {
    const int j = bid;
    STAMP(1, 0);
    const int h = j >> 2, cq = j & 3;
    const int sb = tid >> 7, sd = tid & 127;
    const int p0 = (sb * NH + h) * NSPLIT;
    const f32x4 ml0 = *(const f32x4*)(ws + WS_PML + (size_t)p0 * 8), ml1 = *(const f32x4*)(ws + WS_PML + (size_t)p0 * 8 + 16);
    float po[4];
#pragma unroll
    for (int s = 0; s < 4; ++s) po[s] = ((const float*)(ws + WS_PO))[(size_t)(p0 + s) * DH + sd];
    const int cg = tid & 15, rg0 = tid >> 4;
    u32x4 wv[8];
#pragma unroll
    for (int i = 0; i < 8; ++i) wv[i] = __builtin_nontemporal_load((const u32x4*)(wo + (size_t)(h * DH + rg0 + 16 * i) * DM + cq * 128 + cg * 8));
    const unsigned short xr = x[((tid >> 3) & 1) * DM + (j & 63) * 8 + (tid & 7)];
    __builtin_amdgcn_sched_barrier(0);
    {
      const float M = fmaxf(fmaxf(ml0[0], ml0[2]), fmaxf(ml1[0], ml1[2]));
      const float w0 = __builtin_amdgcn_exp2f(ml0[0] - M), w1 = __builtin_amdgcn_exp2f(ml0[2] - M);
      const float w2 = __builtin_amdgcn_exp2f(ml1[0] - M), w3 = __builtin_amdgcn_exp2f(ml1[2] - M);
      const float L = fmaf(w3, ml1[3], fmaf(w2, ml1[1], fmaf(w1, ml0[3], w0 * ml0[1])));
      const float o = fmaf(w3, po[3], fmaf(w2, po[2], fmaf(w1, po[1], w0 * po[0])));
      os[tid] = rbf(o / L);
    }
    __syncthreads();
    {
      float a[16];
#pragma unroll
      for (int k = 0; k < 16; ++k) a[k] = 0.f;
#pragma unroll
      for (int i = 0; i < 8; ++i) {
        const float o0 = os[rg0 + 16 * i], o1 = os[DH + rg0 + 16 * i];
#pragma unroll
        for (int k = 0; k < 4; ++k) {
          a[2 * k] = fmaf(o0, lo16(wv[i][k]), a[2 * k]); a[2 * k + 1] = fmaf(o0, hi16(wv[i][k]), a[2 * k + 1]);
          a[8 + 2 * k] = fmaf(o1, lo16(wv[i][k]), a[8 + 2 * k]); a[8 + 2 * k + 1] = fmaf(o1, hi16(wv[i][k]), a[8 + 2 * k + 1]);
        }
      }
      TSTEP(8, lane & 32, x32)
      TSTEP(4, lane & 16, x16)
      *(f32x4*)(red + wave * 256 + (lane >> 5) * 128 + cg * 8 + ((lane >> 4) & 1) * 4) = f32x4{a[0], a[1], a[2], a[3]};
    }
    __syncthreads();
    {
      const float y = (red[tid] + red[256 + tid]) + (red[512 + tid] + red[768 + tid]);
      const int col = cq * 128 + sd;
      __hip_atomic_store(g0 + (((size_t)(col >> 3) * NH + h) * 2 + sb) * 8 + (col & 7), (1ull << 32) | (u64)__float_as_uint(y), RLX_AGENT);
    }
    STAMP(1, 6);
    if (j < 64) {
#pragma unroll 1
      for (int pass = PASS0; pass < 2; ++pass) {
      asm volatile("" : "+s"(pass));
      u64 a0, a1; unsigned spins = 0;
      do {
        a0 = __hip_atomic_load(g0 + (size_t)j * 512 + tid, RLX_AGENT);
        a1 = __hip_atomic_load(g0 + (size_t)j * 512 + 256 + tid, RLX_AGENT);
      } while (pass && (((a0 & a1) >> 32) != 1ull) && ++spins < SPIN_CAP);
      float s = __uint_as_float((unsigned)a0) + __uint_as_float((unsigned)a1);
      s = xrow_sum(s);
      if (lane < 16) os[wave * 16 + lane] = s;
      __syncthreads();
      if (tid < 16) {
        const float t = (os[tid] + os[16 + tid]) + (os[32 + tid] + os[48 + tid]);
        const float hv = rbf(bf2f(xr) + rbf(t));
        const float other = __builtin_bit_cast(float, __builtin_amdgcn_mov_dpp(__builtin_bit_cast(int, hv), XOR8, 0xf, 0xf, true));
        if (pass && tid < 8) __hip_atomic_store(g1 + j * 8 + tid, (1ull << 32) | (__float_as_uint(other) & 0xffff0000u) | (__float_as_uint(hv) >> 16), RLX_AGENT);
      }
      __syncthreads();
      }
      STAMP(1, 1);
    } else {
      const int e = j - 64, jj = (e & 7) * 8 + (e >> 3);
      u32x4 d[7];
#pragma unroll
      for (int i = 0; i < 7; ++i) d[i] = __builtin_nontemporal_load((const u32x4*)(wd + (size_t)(wave * 448 + lane + 64 * i) * DM + jj * 8));
      if (tid < 8) abuf[NB * FF + tid] = 0;
      unsigned char* wl = wst + wave * 7168;
#pragma unroll
      for (int i = 0; i < 7; ++i) *(u32x4*)(wl + (lane + 64 * i) * 16) = d[i];
      asm volatile("s_waitcnt lgkmcnt(0)" ::: "memory");
      u32x4 bf[14];
      {
        const unsigned char* p = wl + (8 * (lane >> 4) + ((lane >> 2) & 3)) * 16 + (lane & 3) * 8;
#pragma unroll
        for (int s = 0; s < 14; ++s) bf[s] = tr_frag(p + s * 512, 64);
      }
      u64 av[7], hv; unsigned spins = 0; bool ok;
      do {
        ok = true;
#pragma unroll
        for (int i = 0; i < 7; ++i) { av[i] = __hip_atomic_load(g2 + tid + 256 * i, RLX_AGENT); }
        hv = __hip_atomic_load(g1 + jj * 8 + (tid & 7), RLX_AGENT);
#pragma unroll
        for (int i = 0; i < 7; ++i) ok &= (av[i] >> 32) == 1ull;
        ok &= (hv >> 32) == 1ull;
      } while (!ok && ++spins < SPIN_CAP);
#pragma unroll
      for (int i = 0; i < 7; ++i) { abuf[tid + 256 * i] = (unsigned short)av[i]; abuf[FF + tid + 256 * i] = (unsigned short)(av[i] >> 16); }
      __syncthreads();
      {
        const int m = lane & 15;
        const unsigned short* ap = m < 2 ? abuf + m * FF + wave * 448 + 8 * (lane >> 4) : abuf + NB * FF;
        const int st = m < 2 ? 32 : 0;
        f32x4 c0 = {0, 0, 0, 0}, c1 = {0, 0, 0, 0};
#pragma unroll
        for (int s = 0; s < 14; s += 2) {
          c0 = mfma16(*(const u32x4*)(ap + s * st), bf[s], c0);
          c1 = mfma16(*(const u32x4*)(ap + (s + 1) * st), bf[s + 1], c1);
        }
        c0 += c1;
        if (lane < 8) { red[wave * 16 + lane] = c0[0]; red[wave * 16 + 8 + lane] = c0[1]; }
      }
      __syncthreads();
      if (tid < 16) {
        const float s = (red[tid] + red[16 + tid]) + (red[32 + tid] + red[48 + tid]);
        const float hres = (tid >> 3) ? hi16((unsigned)hv) : lo16((unsigned)hv);
        out[(tid >> 3) * DM + jj * 8 + (tid & 7)] = f2bf(hres + rbf(s));
      }
    }
  } else {
    const int e = bid - 128, j = e < 96 ? ((e >> 5) * 8 + (e & 7)) * 4 + ((e >> 3) & 3) : (24 + ((e - 96) & 3)) * 4 + ((e - 96) >> 2);
    u32x4 g[4], u[4];
#pragma unroll
    for (int i = 0; i < 4; ++i) {
      const size_t off = (size_t)(wave * 128 + 32 * i + (lane >> 1)) * FF + j * 16 + (lane & 1) * 8;
      g[i] = __builtin_nontemporal_load((const u32x4*)(wg + off));
      u[i] = __builtin_nontemporal_load((const u32x4*)(wu + off));
    }
    if (tid < 8) abuf[NB * FF + tid] = 0;
    unsigned char* wl = wst + wave * 8192;
#pragma unroll
    for (int i = 0; i < 4; ++i) {
      *(u32x4*)(wl + (32 * i) * 32 + lane * 16) = g[i];
      *(u32x4*)(wl + 4096 + (32 * i) * 32 + lane * 16) = u[i];
    }
    asm volatile("s_waitcnt lgkmcnt(0)" ::: "memory");
    u32x4 bg[4], bu[4];
    {
      const unsigned char* p = wl + (8 * (lane >> 4) + ((lane >> 2) & 3)) * 32 + (lane & 3) * 8;
#pragma unroll
      for (int s = 0; s < 4; ++s) { bg[s] = tr_frag(p + s * 1024, 128); bu[s] = tr_frag(p + 4096 + s * 1024, 128); }
    }
    u64 a0, a1;
    {
      unsigned spins = 0;
      do {
        a0 = __hip_atomic_load(g1 + tid, RLX_AGENT);
        a1 = __hip_atomic_load(g1 + 256 + tid, RLX_AGENT);
      } while ((((a0 & a1) >> 32) != 1ull) && ++spins < SPIN_CAP);
      hs[tid] = lo16((unsigned)a0); hs[DM + tid] = hi16((unsigned)a0);
      hs[256 + tid] = lo16((unsigned)a1); hs[DM + 256 + tid] = hi16((unsigned)a1);
    }
    __syncthreads();
    STAMP(1, 2);
    {
      const f32x4 p0 = *(const f32x4*)(hs + lane * 8), p1 = *(const f32x4*)(hs + lane * 8 + 4);
      const f32x4 r0 = *(const f32x4*)(hs + DM + lane * 8), r1 = *(const f32x4*)(hs + DM + lane * 8 + 4);
      float s0 = ((p0[0] + p0[1]) + (p0[2] + p0[3])) + ((p1[0] + p1[1]) + (p1[2] + p1[3]));
      float s1 = ((r0[0] + r0[1]) + (r0[2] + r0[3])) + ((r1[0] + r1[1]) + (r1[2] + r1[3]));
      s0 = wave_sum(s0); s1 = wave_sum(s1);
      const float mean0 = s0 * (1.0f / 512.0f), mean1 = s1 * (1.0f / 512.0f);
      float q0 = 0.f, q1 = 0.f;
#pragma unroll
      for (int i = 0; i < 4; ++i) {
        float d0 = p0[i] - mean0, d1 = r0[i] - mean1; q0 += d0 * d0; q1 += d1 * d1;
        d0 = p1[i] - mean0; d1 = r1[i] - mean1; q0 += d0 * d0; q1 += d1 * d1;
      }
      q0 = wave_sum(q0); q1 = wave_sum(q1);
      const float mb0 = rbf(mean0), mb1 = rbf(mean1);
      const float den0 = rbf(sqrtf(rbf(rbf(q0 * (1.0f / 512.0f)) + 1e-5f)));
      const float den1 = rbf(sqrtf(rbf(rbf(q1 * (1.0f / 512.0f)) + 1e-5f)));
      abuf[tid] = f2bf(rbf(lo16((unsigned)a0) - mb0) / den0);
      abuf[256 + tid] = f2bf(rbf(lo16((unsigned)a1) - mb0) / den0);
      abuf[DM + tid] = f2bf(rbf(hi16((unsigned)a0) - mb1) / den1);
      abuf[DM + 256 + tid] = f2bf(rbf(hi16((unsigned)a1) - mb1) / den1);
    }
    __syncthreads();
    {
      const int m = lane & 15;
      const unsigned short* ap = m < 2 ? abuf + m * DM + wave * 128 + 8 * (lane >> 4) : abuf + NB * FF;
      const int st = m < 2 ? 32 : 0;
      f32x4 cg = {0, 0, 0, 0}, cu = {0, 0, 0, 0};
#pragma unroll
      for (int s = 0; s < 4; ++s) {
        const u32x4 af = *(const u32x4*)(ap + s * st);
        cg = mfma16(af, bg[s], cg); cu = mfma16(af, bu[s], cu);
      }
      if (lane < 16) *(f32x4*)(red + (wave * 16 + lane) * 4) = f32x4{cg[0], cu[0], cg[1], cu[1]};
    }
    __syncthreads();
    if (tid < 16) {
      const f32x4 t = (*(const f32x4*)(red + tid * 4) + *(const f32x4*)(red + (16 + tid) * 4)) + (*(const f32x4*)(red + (32 + tid) * 4) + *(const f32x4*)(red + (48 + tid) * 4));
      const float g0v = rbf(t[0]), u0 = rbf(t[1]), g1v = rbf(t[2]), u1 = rbf(t[3]);
      const unsigned short o0 = f2bf(rbf(g0v / (1.0f + __expf(-g0v))) * u0), o1 = f2bf(rbf(g1v / (1.0f + __expf(-g1v))) * u1);
      __hip_atomic_store(g2 + j * 16 + tid, (1ull << 32) | ((unsigned)o1 << 16) | (unsigned)o0, RLX_AGENT);
    }
    STAMP(1, 3);
  }
}

extern "C" void kernel_launch(void* const* d_in, const int* in_sizes, int n_in,
                              void* d_out, int out_size, void* d_ws, size_t ws_size,
                              hipStream_t stream) {
  const unsigned short* x = (const unsigned short*)d_in[0];
  const unsigned short* kc = (const unsigned short*)d_in[1];
  const unsigned short* vc = (const unsigned short*)d_in[2];
  const unsigned short* wq = (const unsigned short*)d_in[3];
  const unsigned short* wo = (const unsigned short*)d_in[4];
  const unsigned short* wg = (const unsigned short*)d_in[6];
  const unsigned short* wu = (const unsigned short*)d_in[7];
  const unsigned short* wd = (const unsigned short*)d_in[8];
  unsigned char* ws = (unsigned char*)d_ws;
  k_attn<<<256, NTHR, 0, stream>>>(x, kc, vc, wq, ws);
  k_tail<<<TAIL_BLOCKS, 256, 0, stream>>>(x, wo, wg, wu, wd, ws, (unsigned short*)d_out);
}
```

```cpp
#define DEP 2
#define PRE 1
#define LBAR 1
#define PASS0 1
#define S2_DELAY 0
#include <hip/hip_runtime.h>
#include <hip/hip_bf16.h>
#include <stdint.h>

typedef unsigned u32x4 __attribute__((ext_vector_type(4)));
typedef float f32x4 __attribute__((ext_vector_type(4)));
typedef __bf16 bf16x2 __attribute__((ext_vector_type(2)));
typedef unsigned long long u64;

constexpr int NB = 2, DM = 512, NH = 32, SK = 16384, DH = 128, FF = 1792;
constexpr int NSPLIT = 4;
constexpr int KPS = SK / NSPLIT;
#define NWV 8
constexpr int NWAVE = NWV, NTHR = NWAVE * 64;
constexpr int QROWS = 512 / (NTHR / 16);
constexpr int SLOT = NWAVE * 256;
constexpr int PAGE = 16;
constexpr int NPAGE = KPS / NWAVE / PAGE;
constexpr int NPART = NB * NH * NSPLIT;

__device__ __forceinline__ float bf2f(unsigned short b) { return __builtin_bit_cast(float, (unsigned)b << 16); }
__device__ __forceinline__ float rbf(float x) { return __bfloat162float(__float2bfloat16(x)); }
__device__ __forceinline__ unsigned short f2bf(float x) { return __builtin_bit_cast(unsigned short, __float2bfloat16(x)); }
__device__ __forceinline__ float lo16(unsigned u) { return __builtin_bit_cast(float, u << 16); }
__device__ __forceinline__ float hi16(unsigned u) { return __builtin_bit_cast(float, u & 0xffff0000u); }
__device__ __forceinline__ float dot2(unsigned k, unsigned q, float acc) {
  return __builtin_amdgcn_fdot2_f32_bf16(__builtin_bit_cast(bf16x2, k), __builtin_bit_cast(bf16x2, q), acc, false);
}
template <int CTRL> __device__ __forceinline__ float dpp(float x) {
  return __builtin_bit_cast(float, __builtin_amdgcn_mov_dpp(__builtin_bit_cast(int, x), CTRL, 0xf, 0xf, true));
}
constexpr int XOR1 = 0xB1, XOR2 = 0x4E, XOR7 = 0x141, XOR8 = 0x128, ROR4 = 0x124;
__device__ __forceinline__ float row16_sum(float x) {
  x += dpp<XOR1>(x); x += dpp<XOR2>(x); x += dpp<XOR7>(x); x += dpp<XOR8>(x); return x;
}
__device__ __forceinline__ float xrow_sum(float x) {
  auto s = __builtin_amdgcn_permlane16_swap(__float_as_uint(x), __float_as_uint(x), false, false);
  x = __uint_as_float(s[0]) + __uint_as_float(s[1]);
  auto t = __builtin_amdgcn_permlane32_swap(__float_as_uint(x), __float_as_uint(x), false, false);
  return __uint_as_float(t[0]) + __uint_as_float(t[1]);
}
__device__ __forceinline__ float xrow_max(float x) {
  auto s = __builtin_amdgcn_permlane16_swap(__float_as_uint(x), __float_as_uint(x), false, false);
  x = fmaxf(__uint_as_float(s[0]), __uint_as_float(s[1]));
  auto t = __builtin_amdgcn_permlane32_swap(__float_as_uint(x), __float_as_uint(x), false, false);
  return fmaxf(__uint_as_float(t[0]), __uint_as_float(t[1]));
}
__device__ __forceinline__ float wave_sum(float x) { return xrow_sum(row16_sum(x)); }

#define STAMP(k, i)
constexpr size_t WS_G0 = 0;
constexpr size_t WS_G1 = 262144;
constexpr size_t WS_G2 = WS_G1 + 4096;
constexpr size_t WS_GEND = WS_G2 + FF * 8;
constexpr size_t WS_Q = 512 * 1024;
constexpr size_t WS_PML = WS_Q + 16384;
constexpr size_t WS_PO = WS_PML + 2048;
constexpr size_t WS_GU = 1u << 20;
constexpr size_t WS_DT = 5u << 20;

__device__ __forceinline__ void ln8(const u32x4 xr, float (&v)[8]) {
#pragma unroll
  for (int k = 0; k < 4; ++k) { v[2 * k] = lo16(xr[k]); v[2 * k + 1] = hi16(xr[k]); }
  float s = ((v[0] + v[1]) + (v[2] + v[3])) + ((v[4] + v[5]) + (v[6] + v[7]));
  s = wave_sum(s);
  const float mean = s * (1.0f / 512.0f);
  float ss = 0.f;
#pragma unroll
  for (int i = 0; i < 8; ++i) { const float d = v[i] - mean; ss += d * d; }
  ss = wave_sum(ss);
  const float var = rbf(ss * (1.0f / 512.0f)), mb = rbf(mean);
  const float den = rbf(sqrtf(rbf(var + 1e-5f)));
#pragma unroll
  for (int i = 0; i < 8; ++i) v[i] = rbf(rbf(v[i] - mb) / den);
}

__global__ void __launch_bounds__(NTHR, NWAVE / 4) k_attn(const unsigned short* __restrict__ x, const unsigned short* __restrict__ kc,
                                                  const unsigned short* __restrict__ vc, const unsigned short* __restrict__ wq,
                                                  unsigned char* __restrict__ ws) {
  __shared__ __attribute__((aligned(16))) unsigned char smem[NWAVE * 2048 + NWAVE * 512 + NWAVE * 132 * 4];
  float* red = (float*)(smem + NWAVE * 2048);
  float* red2 = (float*)(smem + NWAVE * 2048 + NWAVE * 512);
  const int tid = threadIdx.x, lane = tid & 63, wave = __builtin_amdgcn_readfirstlane(tid >> 6), r = lane >> 4, c = lane & 15;
  const int bid = blockIdx.x;
  const int b = bid >> 7, h = (bid >> 2) & 31, sp = bid & 3;
  STAMP(0, 0);
  const u32x4 xr = *(const u32x4*)(x + b * DM + lane * 8);
  const int rg = tid >> 4;
  u32x4 w[QROWS];
  {
    const u32x4* wp = (const u32x4*)(wq + (size_t)h * DH) + c;
#pragma unroll
    for (int i = 0; i < QROWS; ++i) w[i] = __builtin_nontemporal_load(wp + (size_t)(rg + (NTHR / 16) * i) * (NH * DH / 8));
  }
  __builtin_amdgcn_sched_barrier(0);
  const size_t base = ((size_t)(b * NH + h) * SK + (size_t)sp * KPS) * DH;
  const u32x4* kp = (const u32x4*)(kc + base) + wave * 256 + r * 16 + c;
  const u32x4* vp = (const u32x4*)(vc + base) + wave * 256 + r * 16 + c;
  constexpr int DEPTH = DEP;
  u32x4 Kb[DEPTH][4], Vb[DEPTH][4];
#pragma unroll
  for (int d = 0; d < PRE; ++d)
#pragma unroll
    for (int t = 0; t < 4; ++t) {
      Kb[d][t] = __builtin_nontemporal_load(kp + d * SLOT + t * 64);
      Vb[d][t] = __builtin_nontemporal_load(vp + d * SLOT + t * 64);
    }
  __builtin_amdgcn_sched_barrier(0);
  float* lnw = (float*)smem + wave * 512;
  {
    float v[8];
    ln8(xr, v);
    *(f32x4*)(lnw + lane * 8) = f32x4{v[0], v[1], v[2], v[3]};
    *(f32x4*)(lnw + lane * 8 + 4) = f32x4{v[4], v[5], v[6], v[7]};
  }
  asm volatile("s_waitcnt lgkmcnt(0)" ::: "memory");
  {
    float a[8];
#pragma unroll
    for (int k = 0; k < 8; ++k) a[k] = 0.f;
#pragma unroll
    for (int i = 0; i < QROWS; ++i) {
      const float xl = lnw[rg + (NTHR / 16) * i];
#pragma unroll
      for (int k = 0; k < 4; ++k) { a[2 * k] = fmaf(xl, lo16(w[i][k]), a[2 * k]); a[2 * k + 1] = fmaf(xl, hi16(w[i][k]), a[2 * k + 1]); }
    }
#pragma unroll
    for (int k = 0; k < 8; ++k) a[k] = xrow_sum(a[k]);
    if (r == 0) {
      *(f32x4*)(red + wave * 128 + c * 8) = f32x4{a[0], a[1], a[2], a[3]};
      *(f32x4*)(red + wave * 128 + c * 8 + 4) = f32x4{a[4], a[5], a[6], a[7]};
    }
  }
  __syncthreads();
  u32x4 q;
  {
    f32x4 s0 = {0, 0, 0, 0}, s1 = {0, 0, 0, 0};
#pragma unroll
    for (int g = 0; g < NWAVE; ++g) { s0 += *(const f32x4*)(red + g * 128 + c * 8); s1 += *(const f32x4*)(red + g * 128 + c * 8 + 4); }
    q[0] = (unsigned)f2bf(s0[0]) | ((unsigned)f2bf(s0[1]) << 16); q[1] = (unsigned)f2bf(s0[2]) | ((unsigned)f2bf(s0[3]) << 16);
    q[2] = (unsigned)f2bf(s1[0]) | ((unsigned)f2bf(s1[1]) << 16); q[3] = (unsigned)f2bf(s1[2]) | ((unsigned)f2bf(s1[3]) << 16);
  }
  STAMP(0, 1);
#pragma unroll
  for (int d = PRE; d < DEPTH; ++d)
#pragma unroll
    for (int t = 0; t < 4; ++t) {
      Kb[d][t] = __builtin_nontemporal_load(kp + d * SLOT + t * 64);
      Vb[d][t] = __builtin_nontemporal_load(vp + d * SLOT + t * 64);
    }
  __builtin_amdgcn_sched_barrier(0);
  {
    const int i = bid * NTHR + tid;
    if (i < (int)(WS_GEND / 16)) ((u32x4*)ws)[i] = u32x4{0u, 0u, 0u, 0u};
  }
  __builtin_amdgcn_sched_barrier(0);
  STAMP(0, 5);
  constexpr float SCALE = 0.08838834764831845f, L2E = 1.4426950408889634f;
  float m = -INFINITY, l = 0.f, acc[8];
#pragma unroll
  for (int k = 0; k < 8; ++k) acc[k] = 0.f;
  constexpr int NIT = NPAGE / DEPTH;
  for (int it = 0; it < NIT; ++it) {
    const bool more = it + 1 < NIT;
    const int off = more ? (it + 1) * DEPTH * SLOT : -(wave * 256 + r * 16 + c);
    const int sd = more ? SLOT : 0, st = more ? 64 : 0;
#pragma unroll
    for (int d = 0; d < DEPTH; ++d) {
      float s[4];
#pragma unroll
      for (int t = 0; t < 4; ++t) {
        float dd = 0.f;
#pragma unroll
        for (int k = 0; k < 4; ++k) dd = dot2(Kb[d][t][k], q[k], dd);
        dd = row16_sum(dd);
        s[t] = rbf(rbf(dd) * SCALE) * L2E;
      }
      const float pm = fmaxf(fmaxf(s[0], s[1]), fmaxf(s[2], s[3]));
      const float mn = fmaxf(m, pm);
      const float alpha = __builtin_amdgcn_exp2f(m - mn);
      m = mn;
      float pp[4];
#pragma unroll
      for (int t = 0; t < 4; ++t) pp[t] = __builtin_amdgcn_exp2f(s[t] - mn);
      l = fmaf(l, alpha, (pp[0] + pp[1]) + (pp[2] + pp[3]));
#pragma unroll
      for (int k = 0; k < 8; ++k) acc[k] *= alpha;
#pragma unroll
      for (int t = 0; t < 4; ++t)
#pragma unroll
        for (int k = 0; k < 4; ++k) {
          acc[2 * k] = fmaf(pp[t], lo16(Vb[d][t][k]), acc[2 * k]);
          acc[2 * k + 1] = fmaf(pp[t], hi16(Vb[d][t][k]), acc[2 * k + 1]);
        }
      asm volatile("" : "+v"(acc[0]), "+v"(acc[1]), "+v"(acc[2]), "+v"(acc[3]), "+v"(acc[4]), "+v"(acc[5]), "+v"(acc[6]), "+v"(acc[7]));
      __builtin_amdgcn_sched_barrier(0);
#pragma unroll
      for (int t = 0; t < 4; ++t) {
        Kb[d][t] = __builtin_nontemporal_load(kp + off + d * sd + t * st);
        Vb[d][t] = __builtin_nontemporal_load(vp + off + d * sd + t * st);
      }
      __builtin_amdgcn_sched_barrier(0);
    }
    __builtin_amdgcn_s_barrier();
  }
  STAMP(0, 2);
  {
    const float M = xrow_max(m);
    const float wgt = __builtin_amdgcn_exp2f(m - M);
    const float L = xrow_sum(l * wgt);
#pragma unroll
    for (int k = 0; k < 8; ++k) acc[k] = xrow_sum(acc[k] * wgt);
    float* rec = red2 + wave * 132;
    if (r == 0) {
      *(f32x4*)(rec + 4 + 8 * c) = f32x4{acc[0], acc[1], acc[2], acc[3]};
      *(f32x4*)(rec + 8 + 8 * c) = f32x4{acc[4], acc[5], acc[6], acc[7]};
      if (c == 0) { rec[0] = M; rec[1] = L; }
    }
  }
  __syncthreads();
  if (tid < DH) {
    float M = -INFINITY;
#pragma unroll
    for (int g = 0; g < NWAVE; ++g) M = fmaxf(M, red2[g * 132]);
    float L = 0.f, o = 0.f;
#pragma unroll
    for (int g = 0; g < NWAVE; ++g) {
      const float wgt = __builtin_amdgcn_exp2f(red2[g * 132] - M);
      L = fmaf(wgt, red2[g * 132 + 1], L);
      o = fmaf(wgt, red2[g * 132 + 4 + tid], o);
    }
    ((float*)(ws + WS_PO))[(size_t)bid * DH + tid] = o;
    if (tid == 0) ((float2*)(ws + WS_PML))[bid] = make_float2(M, L);
  }
  STAMP(0, 3);
}

#define RLX_AGENT __ATOMIC_RELAXED, __HIP_MEMORY_SCOPE_AGENT
constexpr unsigned SPIN_CAP = 1u << 22;

constexpr int TAIL_BLOCKS = 128 + 112;
#define TSTEP(N, BIT, XCHG)                                                                       \
  _Pragma("unroll") for (int i_ = 0; i_ < (N); ++i_) {                                            \
    const float keep_ = (BIT) ? a[i_ + (N)] : a[i_], send_ = (BIT) ? a[i_] : a[i_ + (N)];         \
    a[i_] = keep_ + XCHG(send_);                                                                  \
  }
__device__ __forceinline__ float x16(float v) {
  auto s = __builtin_amdgcn_permlane16_swap(__float_as_uint(v), __float_as_uint(v), false, false);
  return __uint_as_float((__lane_id() & 16) ? s[0] : s[1]);
}
__device__ __forceinline__ float x32(float v) {
  auto s = __builtin_amdgcn_permlane32_swap(__float_as_uint(v), __float_as_uint(v), false, false);
  return __uint_as_float((__lane_id() & 32) ? s[0] : s[1]);
}
typedef short s16x4 __attribute__((ext_vector_type(4)));
typedef __bf16 bf16x8 __attribute__((ext_vector_type(8)));
typedef unsigned u32x2 __attribute__((ext_vector_type(2)));
#define LAS __attribute__((address_space(3)))
__device__ __forceinline__ u32x4 tr_frag(const unsigned char* p, int pitch4) {
  const u32x2 a = __builtin_bit_cast(u32x2, __builtin_amdgcn_ds_read_tr16_b64_v4i16((LAS s16x4*)p));
  const u32x2 b = __builtin_bit_cast(u32x2, __builtin_amdgcn_ds_read_tr16_b64_v4i16((LAS s16x4*)(p + pitch4)));
  return u32x4{a[0], a[1], b[0], b[1]};
}
__device__ __forceinline__ f32x4 mfma16(u32x4 a, u32x4 b, f32x4 c) {
  return __builtin_amdgcn_mfma_f32_16x16x32_bf16(__builtin_bit_cast(bf16x8, a), __builtin_bit_cast(bf16x8, b), c, 0, 0, 0);
}
__global__ void __launch_bounds__(256) k_tail(const unsigned short* __restrict__ x, const unsigned short* __restrict__ wo,
                                              const unsigned short* __restrict__ wg, const unsigned short* __restrict__ wu,
                                              const unsigned short* __restrict__ wd, unsigned char* ws, unsigned short* __restrict__ out) {
  __shared__ __attribute__((aligned(16))) float hs[NB * DM];
  __shared__ __attribute__((aligned(16))) float red[4 * 256];
  __shared__ __attribute__((aligned(16))) float os[NB * DH];
  __shared__ __attribute__((aligned(16))) unsigned char wst[32768 + 64];
  __shared__ __attribute__((aligned(16))) unsigned short abuf[NB * FF + 8];
  const int tid = threadIdx.x, lane = tid & 63, wave = tid >> 6, bid = blockIdx.x;
  u64* g0 = (u64*)(ws + WS_G0);
  u64* g1 = (u64*)(ws + WS_G1);
  u64* g2 = (u64*)(ws + WS_G2);
  if (bid < 128) {
    const int j = bid;
    STAMP(1, 0);
    const int h = j >> 2, cq = j & 3;
    const int sb = tid >> 7, sd = tid & 127;
    const int p0 = (sb * NH + h) * NSPLIT;
    const f32x4 ml0 = *(const f32x4*)(ws + WS_PML + (size_t)p0 * 8), ml1 = *(const f32x4*)(ws + WS_PML + (size_t)p0 * 8 + 16);
    float po[4];
#pragma unroll
    for (int s = 0; s < 4; ++s) po[s] = ((const float*)(ws + WS_PO))[(size_t)(p0 + s) * DH + sd];
    const int cg = tid & 15, rg0 = tid >> 4;
    u32x4 wv[8];
#pragma unroll
    for (int i = 0; i < 8; ++i) wv[i] = __builtin_nontemporal_load((const u32x4*)(wo + (size_t)(h * DH + rg0 + 16 * i) * DM + cq * 128 + cg * 8));
    const unsigned short xr = x[((tid >> 3) & 1) * DM + (j & 63) * 8 + (tid & 7)];
    __builtin_amdgcn_sched_barrier(0);
    {
      const float M = fmaxf(fmaxf(ml0[0], ml0[2]), fmaxf(ml1[0], ml1[2]));
      const float w0 = __builtin_amdgcn_exp2f(ml0[0] - M), w1 = __builtin_amdgcn_exp2f(ml0[2] - M);
      const float w2 = __builtin_amdgcn_exp2f(ml1[0] - M), w3 = __builtin_amdgcn_exp2f(ml1[2] - M);
      const float L = fmaf(w3, ml1[3], fmaf(w2, ml1[1], fmaf(w1, ml0[3], w0 * ml0[1])));
      const float o = fmaf(w3, po[3], fmaf(w2, po[2], fmaf(w1, po[1], w0 * po[0])));
      os[tid] = rbf(o / L);
    }
    __syncthreads();
    {
      float a[16];
#pragma unroll
      for (int k = 0; k < 16; ++k) a[k] = 0.f;
#pragma unroll
      for (int i = 0; i < 8; ++i) {
        const float o0 = os[rg0 + 16 * i], o1 = os[DH + rg0 + 16 * i];
#pragma unroll
        for (int k = 0; k < 4; ++k) {
          a[2 * k] = fmaf(o0, lo16(wv[i][k]), a[2 * k]); a[2 * k + 1] = fmaf(o0, hi16(wv[i][k]), a[2 * k + 1]);
          a[8 + 2 * k] = fmaf(o1, lo16(wv[i][k]), a[8 + 2 * k]); a[8 + 2 * k + 1] = fmaf(o1, hi16(wv[i][k]), a[8 + 2 * k + 1]);
        }
      }
      TSTEP(8, lane & 32, x32)
      TSTEP(4, lane & 16, x16)
      *(f32x4*)(red + wave * 256 + (lane >> 5) * 128 + cg * 8 + ((lane >> 4) & 1) * 4) = f32x4{a[0], a[1], a[2], a[3]};
    }
    __syncthreads();
    {
      const float y = (red[tid] + red[256 + tid]) + (red[512 + tid] + red[768 + tid]);
      const int col = cq * 128 + sd;
      __hip_atomic_store(g0 + (((size_t)(col >> 3) * NH + h) * 2 + sb) * 8 + (col & 7), (1ull << 32) | (u64)__float_as_uint(y), RLX_AGENT);
    }
    STAMP(1, 6);
    if (j < 64) {
#pragma unroll 1
      for (int pass = PASS0; pass < 2; ++pass) {
      asm volatile("" : "+s"(pass));
      u64 a0, a1; unsigned spins = 0;
      do {
        a0 = __hip_atomic_load(g0 + (size_t)j * 512 + tid, RLX_AGENT);
        a1 = __hip_atomic_load(g0 + (size_t)j * 512 + 256 + tid, RLX_AGENT);
      } while (pass && (((a0 & a1) >> 32) != 1ull) && ++spins < SPIN_CAP);
      float s = __uint_as_float((unsigned)a0) + __uint_as_float((unsigned)a1);
      s = xrow_sum(s);
      if (lane < 16) os[wave * 16 + lane] = s;
      __syncthreads();
      if (tid < 16) {
        const float t = (os[tid] + os[16 + tid]) + (os[32 + tid] + os[48 + tid]);
        const float hv = rbf(bf2f(xr) + rbf(t));
        const float other = __builtin_bit_cast(float, __builtin_amdgcn_mov_dpp(__builtin_bit_cast(int, hv), XOR8, 0xf, 0xf, true));
        if (pass && tid < 8) __hip_atomic_store(g1 + j * 8 + tid, (1ull << 32) | (__float_as_uint(other) & 0xffff0000u) | (__float_as_uint(hv) >> 16), RLX_AGENT);
      }
      __syncthreads();
      }
      STAMP(1, 1);
    } else {
      const int e = j - 64, jj = (e & 7) * 8 + (e >> 3);
      u32x4 d[7];
#pragma unroll
      for (int i = 0; i < 7; ++i) d[i] = __builtin_nontemporal_load((const u32x4*)(wd + (size_t)(wave * 448 + lane + 64 * i) * DM + jj * 8));
      if (tid < 8) abuf[NB * FF + tid] = 0;
      unsigned char* wl = wst + wave * 7168;
#pragma unroll
      for (int i = 0; i < 7; ++i) *(u32x4*)(wl + (lane + 64 * i) * 16) = d[i];
      asm volatile("s_waitcnt lgkmcnt(0)" ::: "memory");
      u32x4 bf[14];
      {
        const unsigned char* p = wl + (8 * (lane >> 4) + ((lane >> 2) & 3)) * 16 + (lane & 3) * 8;
#pragma unroll
        for (int s = 0; s < 14; ++s) bf[s] = tr_frag(p + s * 512, 64);
      }
      u64 av[7], hv; unsigned spins = 0; bool ok;
      do {
        ok = true;
#pragma unroll
        for (int i = 0; i < 7; ++i) { av[i] = __hip_atomic_load(g2 + tid + 256 * i, RLX_AGENT); }
        hv = __hip_atomic_load(g1 + jj * 8 + (tid & 7), RLX_AGENT);
#pragma unroll
        for (int i = 0; i < 7; ++i) ok &= (av[i] >> 32) == 1ull;
        ok &= (hv >> 32) == 1ull;
      } while (!ok && ++spins < SPIN_CAP);
#pragma unroll
      for (int i = 0; i < 7; ++i) { abuf[tid + 256 * i] = (unsigned short)av[i]; abuf[FF + tid + 256 * i] = (unsigned short)(av[i] >> 16); }
      __syncthreads();
      {
        const int m = lane & 15;
        const unsigned short* ap = m < 2 ? abuf + m * FF + wave * 448 + 8 * (lane >> 4) : abuf + NB * FF;
        const int st = m < 2 ? 32 : 0;
        f32x4 c0 = {0, 0, 0, 0}, c1 = {0, 0, 0, 0};
#pragma unroll
        for (int s = 0; s < 14; s += 2) {
          c0 = mfma16(*(const u32x4*)(ap + s * st), bf[s], c0);
          c1 = mfma16(*(const u32x4*)(ap + (s + 1) * st), bf[s + 1], c1);
        }
        c0 += c1;
        if (lane < 8) { red[wave * 16 + lane] = c0[0]; red[wave * 16 + 8 + lane] = c0[1]; }
      }
      __syncthreads();
      if (tid < 16) {
        const float s = (red[tid] + red[16 + tid]) + (red[32 + tid] + red[48 + tid]);
        const float hres = (tid >> 3) ? hi16((unsigned)hv) : lo16((unsigned)hv);
        out[(tid >> 3) * DM + jj * 8 + (tid & 7)] = f2bf(hres + rbf(s));
      }
    }
  } else {
    const int e = bid - 128, j = e < 96 ? ((e >> 5) * 8 + (e & 7)) * 4 + ((e >> 3) & 3) : (24 + ((e - 96) & 3)) * 4 + ((e - 96) >> 2);
    u32x4 g[4], u[4];
#pragma unroll
    for (int i = 0; i < 4; ++i) {
      const size_t off = (size_t)(wave * 128 + 32 * i + (lane >> 1)) * FF + j * 16 + (lane & 1) * 8;
      g[i] = __builtin_nontemporal_load((const u32x4*)(wg + off));
      u[i] = __builtin_nontemporal_load((const u32x4*)(wu + off));
    }
    if (tid < 8) abuf[NB * FF + tid] = 0;
    unsigned char* wl = wst + wave * 8192;
#pragma unroll
    for (int i = 0; i < 4; ++i) {
      *(u32x4*)(wl + (32 * i) * 32 + lane * 16) = g[i];
      *(u32x4*)(wl + 4096 + (32 * i) * 32 + lane * 16) = u[i];
    }
    asm volatile("s_waitcnt lgkmcnt(0)" ::: "memory");
    u32x4 bg[4], bu[4];
    {
      const unsigned char* p = wl + (8 * (lane >> 4) + ((lane >> 2) & 3)) * 32 + (lane & 3) * 8;
#pragma unroll
      for (int s = 0; s < 4; ++s) { bg[s] = tr_frag(p + s * 1024, 128); bu[s] = tr_frag(p + 4096 + s * 1024, 128); }
    }
    u64 a0, a1;
    {
      unsigned spins = 0;
      do {
        a0 = __hip_atomic_load(g1 + tid, RLX_AGENT);
        a1 = __hip_atomic_load(g1 + 256 + tid, RLX_AGENT);
      } while ((((a0 & a1) >> 32) != 1ull) && ++spins < SPIN_CAP);
      hs[tid] = lo16((unsigned)a0); hs[DM + tid] = hi16((unsigned)a0);
      hs[256 + tid] = lo16((unsigned)a1); hs[DM + 256 + tid] = hi16((unsigned)a1);
    }
    __syncthreads();
    STAMP(1, 2);
    {
      const f32x4 p0 = *(const f32x4*)(hs + lane * 8), p1 = *(const f32x4*)(hs + lane * 8 + 4);
      const f32x4 r0 = *(const f32x4*)(hs + DM + lane * 8), r1 = *(const f32x4*)(hs + DM + lane * 8 + 4);
      float s0 = ((p0[0] + p0[1]) + (p0[2] + p0[3])) + ((p1[0] + p1[1]) + (p1[2] + p1[3]));
      float s1 = ((r0[0] + r0[1]) + (r0[2] + r0[3])) + ((r1[0] + r1[1]) + (r1[2] + r1[3]));
      s0 = wave_sum(s0); s1 = wave_sum(s1);
      const float mean0 = s0 * (1.0f / 512.0f), mean1 = s1 * (1.0f / 512.0f);
      float q0 = 0.f, q1 = 0.f;
#pragma unroll
      for (int i = 0; i < 4; ++i) {
        float d0 = p0[i] - mean0, d1 = r0[i] - mean1; q0 += d0 * d0; q1 += d1 * d1;
        d0 = p1[i] - mean0; d1 = r1[i] - mean1; q0 += d0 * d0; q1 += d1 * d1;
      }
      q0 = wave_sum(q0); q1 = wave_sum(q1);
      const float mb0 = rbf(mean0), mb1 = rbf(mean1);
      const float den0 = rbf(sqrtf(rbf(rbf(q0 * (1.0f / 512.0f)) + 1e-5f)));
      const float den1 = rbf(sqrtf(rbf(rbf(q1 * (1.0f / 512.0f)) + 1e-5f)));
      abuf[tid] = f2bf(rbf(lo16((unsigned)a0) - mb0) / den0);
      abuf[256 + tid] = f2bf(rbf(lo16((unsigned)a1) - mb0) / den0);
      abuf[DM + tid] = f2bf(rbf(hi16((unsigned)a0) - mb1) / den1);
      abuf[DM + 256 + tid] = f2bf(rbf(hi16((unsigned)a1) - mb1) / den1);
    }
    __syncthreads();
    {
      const int m = lane & 15;
      const unsigned short* ap = m < 2 ? abuf + m * DM + wave * 128 + 8 * (lane >> 4) : abuf + NB * FF;
      const int st = m < 2 ? 32 : 0;
      f32x4 cg = {0, 0, 0, 0}, cu = {0, 0, 0, 0};
#pragma unroll
      for (int s = 0; s < 4; ++s) {
        const u32x4 af = *(const u32x4*)(ap + s * st);
        cg = mfma16(af, bg[s], cg); cu = mfma16(af, bu[s], cu);
      }
      if (lane < 16) *(f32x4*)(red + (wave * 16 + lane) * 4) = f32x4{cg[0], cu[0], cg[1], cu[1]};
    }
    __syncthreads();
    if (tid < 16) {
      const f32x4 t = (*(const f32x4*)(red + tid * 4) + *(const f32x4*)(red + (16 + tid) * 4)) + (*(const f32x4*)(red + (32 + tid) * 4) + *(const f32x4*)(red + (48 + tid) * 4));
      const float g0v = rbf(t[0]), u0 = rbf(t[1]), g1v = rbf(t[2]), u1 = rbf(t[3]);
      const unsigned short o0 = f2bf(rbf(g0v / (1.0f + __expf(-g0v))) * u0), o1 = f2bf(rbf(g1v / (1.0f + __expf(-g1v))) * u1);
      __hip_atomic_store(g2 + j * 16 + tid, (1ull << 32) | ((unsigned)o1 << 16) | (unsigned)o0, RLX_AGENT);
    }
    STAMP(1, 3);
  }
}

extern "C" void kernel_launch(void* const* d_in, const int* in_sizes, int n_in,
                              void* d_out, int out_size, void* d_ws, size_t ws_size,
                              hipStream_t stream) {
  const unsigned short* x = (const unsigned short*)d_in[0];
  const unsigned short* kc = (const unsigned short*)d_in[1];
  const unsigned short* vc = (const unsigned short*)d_in[2];
  const unsigned short* wq = (const unsigned short*)d_in[3];
  const unsigned short* wo = (const unsigned short*)d_in[4];
  const unsigned short* wg = (const unsigned short*)d_in[6];
  const unsigned short* wu = (const unsigned short*)d_in[7];
  const unsigned short* wd = (const unsigned short*)d_in[8];
  unsigned char* ws = (unsigned char*)d_ws;
  k_attn<<<256, NTHR, 0, stream>>>(x, kc, vc, wq, ws);
  k_tail<<<TAIL_BLOCKS, 256, 0, stream>>>(x, wo, wg, wu, wd, ws, (unsigned short*)d_out);
}
```

```cpp
#include <hip/hip_runtime.h>
#include <hip/hip_bf16.h>
#include <stdint.h>

typedef unsigned u32x4 __attribute__((ext_vector_type(4)));
typedef float f32x4 __attribute__((ext_vector_type(4)));
typedef __bf16 bf16x2 __attribute__((ext_vector_type(2)));
typedef unsigned long long u64;

constexpr int NB = 2, DM = 512, NH = 32, SK = 16384, DH = 128, FF = 1792;
constexpr int NSPLIT = 4;
constexpr int KPS = SK / NSPLIT;
constexpr int NWAVE = 8, NTHR = NWAVE * 64;
constexpr int QROWS = 512 / (NTHR / 16);
constexpr int SLOT = NWAVE * 256;
constexpr int PAGE = 16;
constexpr int NPAGE = KPS / NWAVE / PAGE;
constexpr int NPART = NB * NH * NSPLIT;

__device__ __forceinline__ float bf2f(unsigned short b) { return __builtin_bit_cast(float, (unsigned)b << 16); }
__device__ __forceinline__ float rbf(float x) { return __bfloat162float(__float2bfloat16(x)); }
__device__ __forceinline__ unsigned short f2bf(float x) { return __builtin_bit_cast(unsigned short, __float2bfloat16(x)); }
__device__ __forceinline__ float lo16(unsigned u) { return __builtin_bit_cast(float, u << 16); }
__device__ __forceinline__ float hi16(unsigned u) { return __builtin_bit_cast(float, u & 0xffff0000u); }
__device__ __forceinline__ float dot2(unsigned k, unsigned q, float acc) {
  return __builtin_amdgcn_fdot2_f32_bf16(__builtin_bit_cast(bf16x2, k), __builtin_bit_cast(bf16x2, q), acc, false);
}
template <int CTRL> __device__ __forceinline__ float dpp(float x) {
  return __builtin_bit_cast(float, __builtin_amdgcn_mov_dpp(__builtin_bit_cast(int, x), CTRL, 0xf, 0xf, true));
}
constexpr int XOR1 = 0xB1, XOR2 = 0x4E, XOR7 = 0x141, XOR8 = 0x128, ROR4 = 0x124;
__device__ __forceinline__ float row16_sum(float x) {
  x += dpp<XOR1>(x); x += dpp<XOR2>(x); x += dpp<XOR7>(x); x += dpp<XOR8>(x); return x;
}
__device__ __forceinline__ float xrow_sum(float x) {
  auto s = __builtin_amdgcn_permlane16_swap(__float_as_uint(x), __float_as_uint(x), false, false);
  x = __uint_as_float(s[0]) + __uint_as_float(s[1]);
  auto t = __builtin_amdgcn_permlane32_swap(__float_as_uint(x), __float_as_uint(x), false, false);
  return __uint_as_float(t[0]) + __uint_as_float(t[1]);
}
__device__ __forceinline__ float xrow_max(float x) {
  auto s = __builtin_amdgcn_permlane16_swap(__float_as_uint(x), __float_as_uint(x), false, false);
  x = fmaxf(__uint_as_float(s[0]), __uint_as_float(s[1]));
  auto t = __builtin_amdgcn_permlane32_swap(__float_as_uint(x), __float_as_uint(x), false, false);
  return fmaxf(__uint_as_float(t[0]), __uint_as_float(t[1]));
}
__device__ __forceinline__ float wave_sum(float x) { return xrow_sum(row16_sum(x)); }

constexpr size_t WS_G0 = 0;
constexpr size_t WS_G1 = 262144;
constexpr size_t WS_G2 = WS_G1 + 4096;
constexpr size_t WS_GEND = WS_G2 + FF * 8;
constexpr size_t WS_PML = 512 * 1024;
constexpr size_t WS_PO = WS_PML + 2048;

__device__ __forceinline__ void ln8(const u32x4 xr, float (&v)[8]) {
#pragma unroll
  for (int k = 0; k < 4; ++k) { v[2 * k] = lo16(xr[k]); v[2 * k + 1] = hi16(xr[k]); }
  float s = ((v[0] + v[1]) + (v[2] + v[3])) + ((v[4] + v[5]) + (v[6] + v[7]));
  s = wave_sum(s);
  const float mean = s * (1.0f / 512.0f);
  float ss = 0.f;
#pragma unroll
  for (int i = 0; i < 8; ++i) { const float d = v[i] - mean; ss += d * d; }
  ss = wave_sum(ss);
  const float var = rbf(ss * (1.0f / 512.0f)), mb = rbf(mean);
  const float den = rbf(sqrtf(rbf(var + 1e-5f)));
#pragma unroll
  for (int i = 0; i < 8; ++i) v[i] = rbf(rbf(v[i] - mb) / den);
}

__global__ void __launch_bounds__(NTHR, NWAVE / 4) k_attn(const unsigned short* __restrict__ x, const unsigned short* __restrict__ kc,
                                                  const unsigned short* __restrict__ vc, const unsigned short* __restrict__ wq,
                                                  unsigned char* __restrict__ ws) {
  __shared__ __attribute__((aligned(16))) unsigned char smem[NWAVE * 2048 + NWAVE * 512 + NWAVE * 132 * 4];
  float* red = (float*)(smem + NWAVE * 2048);
  float* red2 = (float*)(smem + NWAVE * 2048 + NWAVE * 512);
  const int tid = threadIdx.x, lane = tid & 63, wave = __builtin_amdgcn_readfirstlane(tid >> 6), r = lane >> 4, c = lane & 15;
  const int bid = blockIdx.x;
  const int b = bid >> 7, h = (bid >> 2) & 31, sp = bid & 3;
  const u32x4 xr = *(const u32x4*)(x + b * DM + lane * 8);
  const int rg = tid >> 4;
  u32x4 w[QROWS];
  {
    const u32x4* wp = (const u32x4*)(wq + (size_t)h * DH) + c;
#pragma unroll
    for (int i = 0; i < QROWS; ++i) w[i] = __builtin_nontemporal_load(wp + (size_t)(rg + (NTHR / 16) * i) * (NH * DH / 8));
  }
  __builtin_amdgcn_sched_barrier(0);
  const size_t base = ((size_t)(b * NH + h) * SK + (size_t)sp * KPS) * DH;
  const u32x4* kp = (const u32x4*)(kc + base) + wave * 256 + r * 16 + c;
  const u32x4* vp = (const u32x4*)(vc + base) + wave * 256 + r * 16 + c;
  constexpr int DEPTH = 2;
  u32x4 Kb[DEPTH][4], Vb[DEPTH][4];
#pragma unroll
  for (int d = 0; d < 1; ++d)
#pragma unroll
    for (int t = 0; t < 4; ++t) {
      Kb[d][t] = __builtin_nontemporal_load(kp + d * SLOT + t * 64);
      Vb[d][t] = __builtin_nontemporal_load(vp + d * SLOT + t * 64);
    }
  __builtin_amdgcn_sched_barrier(0);
  float* lnw = (float*)smem + wave * 512;
  {
    float v[8];
    ln8(xr, v);
    *(f32x4*)(lnw + lane * 8) = f32x4{v[0], v[1], v[2], v[3]};
    *(f32x4*)(lnw + lane * 8 + 4) = f32x4{v[4], v[5], v[6], v[7]};
  }
  asm volatile("s_waitcnt lgkmcnt(0)" ::: "memory");
  {
    float a[8];
#pragma unroll
    for (int k = 0; k < 8; ++k) a[k] = 0.f;
#pragma unroll
    for (int i = 0; i < QROWS; ++i) {
      const float xl = lnw[rg + (NTHR / 16) * i];
#pragma unroll
      for (int k = 0; k < 4; ++k) { a[2 * k] = fmaf(xl, lo16(w[i][k]), a[2 * k]); a[2 * k + 1] = fmaf(xl, hi16(w[i][k]), a[2 * k + 1]); }
    }
#pragma unroll
    for (int k = 0; k < 8; ++k) a[k] = xrow_sum(a[k]);
    if (r == 0) {
      *(f32x4*)(red + wave * 128 + c * 8) = f32x4{a[0], a[1], a[2], a[3]};
      *(f32x4*)(red + wave * 128 + c * 8 + 4) = f32x4{a[4], a[5], a[6], a[7]};
    }
  }
  __syncthreads();
  u32x4 q;
  {
    f32x4 s0 = {0, 0, 0, 0}, s1 = {0, 0, 0, 0};
#pragma unroll
    for (int g = 0; g < NWAVE; ++g) { s0 += *(const f32x4*)(red + g * 128 + c * 8); s1 += *(const f32x4*)(red + g * 128 + c * 8 + 4); }
    q[0] = (unsigned)f2bf(s0[0]) | ((unsigned)f2bf(s0[1]) << 16); q[1] = (unsigned)f2bf(s0[2]) | ((unsigned)f2bf(s0[3]) << 16);
    q[2] = (unsigned)f2bf(s1[0]) | ((unsigned)f2bf(s1[1]) << 16); q[3] = (unsigned)f2bf(s1[2]) | ((unsigned)f2bf(s1[3]) << 16);
  }
#pragma unroll
  for (int d = 1; d < DEPTH; ++d)
#pragma unroll
    for (int t = 0; t < 4; ++t) {
      Kb[d][t] = __builtin_nontemporal_load(kp + d * SLOT + t * 64);
      Vb[d][t] = __builtin_nontemporal_load(vp + d * SLOT + t * 64);
    }
  __builtin_amdgcn_sched_barrier(0);
  {
    const int i = bid * NTHR + tid;
    if (i < (int)(WS_GEND / 16)) ((u32x4*)ws)[i] = u32x4{0u, 0u, 0u, 0u};
  }
  __builtin_amdgcn_sched_barrier(0);
  constexpr float SCALE = 0.08838834764831845f, L2E = 1.4426950408889634f;
  float m = -INFINITY, l = 0.f, acc[8];
#pragma unroll
  for (int k = 0; k < 8; ++k) acc[k] = 0.f;
  constexpr int NIT = NPAGE / DEPTH;
  for (int it = 0; it < NIT; ++it) {
    const bool more = it + 1 < NIT;
    const int off = more ? (it + 1) * DEPTH * SLOT : -(wave * 256 + r * 16 + c);
    const int sd = more ? SLOT : 0, st = more ? 64 : 0;
#pragma unroll
    for (int d = 0; d < DEPTH; ++d) {
      float s[4];
#pragma unroll
      for (int t = 0; t < 4; ++t) {
        float dd = 0.f;
#pragma unroll
        for (int k = 0; k < 4; ++k) dd = dot2(Kb[d][t][k], q[k], dd);
        dd = row16_sum(dd);
        s[t] = rbf(rbf(dd) * SCALE) * L2E;
      }
      const float pm = fmaxf(fmaxf(s[0], s[1]), fmaxf(s[2], s[3]));
      const float mn = fmaxf(m, pm);
      const float alpha = __builtin_amdgcn_exp2f(m - mn);
      m = mn;
      float pp[4];
#pragma unroll
      for (int t = 0; t < 4; ++t) pp[t] = __builtin_amdgcn_exp2f(s[t] - mn);
      l = fmaf(l, alpha, (pp[0] + pp[1]) + (pp[2] + pp[3]));
#pragma unroll
      for (int k = 0; k < 8; ++k) acc[k] *= alpha;
#pragma unroll
      for (int t = 0; t < 4; ++t)
#pragma unroll
        for (int k = 0; k < 4; ++k) {
          acc[2 * k] = fmaf(pp[t], lo16(Vb[d][t][k]), acc[2 * k]);
          acc[2 * k + 1] = fmaf(pp[t], hi16(Vb[d][t][k]), acc[2 * k + 1]);
        }
      asm volatile("" : "+v"(acc[0]), "+v"(acc[1]), "+v"(acc[2]), "+v"(acc[3]), "+v"(acc[4]), "+v"(acc[5]), "+v"(acc[6]), "+v"(acc[7]));
      __builtin_amdgcn_sched_barrier(0);
#pragma unroll
      for (int t = 0; t < 4; ++t) {
        Kb[d][t] = __builtin_nontemporal_load(kp + off + d * sd + t * st);
        Vb[d][t] = __builtin_nontemporal_load(vp + off + d * sd + t * st);
      }
      __builtin_amdgcn_sched_barrier(0);
    }
    __builtin_amdgcn_s_barrier();
  }
  {
    const float M = xrow_max(m);
    const float wgt = __builtin_amdgcn_exp2f(m - M);
    const float L = xrow_sum(l * wgt);
#pragma unroll
    for (int k = 0; k < 8; ++k) acc[k] = xrow_sum(acc[k] * wgt);
    float* rec = red2 + wave * 132;
    if (r == 0) {
      *(f32x4*)(rec + 4 + 8 * c) = f32x4{acc[0], acc[1], acc[2], acc[3]};
      *(f32x4*)(rec + 8 + 8 * c) = f32x4{acc[4], acc[5], acc[6], acc[7]};
      if (c == 0) { rec[0] = M; rec[1] = L; }
    }
  }
  __syncthreads();
  if (tid < DH) {
    float M = -INFINITY;
#pragma unroll
    for (int g = 0; g < NWAVE; ++g) M = fmaxf(M, red2[g * 132]);
    float L = 0.f, o = 0.f;
#pragma unroll
    for (int g = 0; g < NWAVE; ++g) {
      const float wgt = __builtin_amdgcn_exp2f(red2[g * 132] - M);
      L = fmaf(wgt, red2[g * 132 + 1], L);
      o = fmaf(wgt, red2[g * 132 + 4 + tid], o);
    }
    ((float*)(ws + WS_PO))[(size_t)bid * DH + tid] = o;
    if (tid == 0) ((float2*)(ws + WS_PML))[bid] = make_float2(M, L);
  }
}

#define RLX_AGENT __ATOMIC_RELAXED, __HIP_MEMORY_SCOPE_AGENT
constexpr unsigned SPIN_CAP = 1u << 20;

constexpr int TAIL_BLOCKS = 128 + 112;
#define TSTEP(N, BIT, XCHG)                                                                       \
  _Pragma("unroll") for (int i_ = 0; i_ < (N); ++i_) {                                            \
    const float keep_ = (BIT) ? a[i_ + (N)] : a[i_], send_ = (BIT) ? a[i_] : a[i_ + (N)];         \
    a[i_] = keep_ + XCHG(send_);                                                                  \
  }
__device__ __forceinline__ float x16(float v) {
  auto s = __builtin_amdgcn_permlane16_swap(__float_as_uint(v), __float_as_uint(v), false, false);
  return __uint_as_float((__lane_id() & 16) ? s[0] : s[1]);
}
__device__ __forceinline__ float x32(float v) {
  auto s = __builtin_amdgcn_permlane32_swap(__float_as_uint(v), __float_as_uint(v), false, false);
  return __uint_as_float((__lane_id() & 32) ? s[0] : s[1]);
}
typedef short s16x4 __attribute__((ext_vector_type(4)));
typedef __bf16 bf16x8 __attribute__((ext_vector_type(8)));
typedef unsigned u32x2 __attribute__((ext_vector_type(2)));
#define LAS __attribute__((address_space(3)))
__device__ __forceinline__ u32x4 tr_frag(const unsigned char* p, int pitch4) {
  const u32x2 a = __builtin_bit_cast(u32x2, __builtin_amdgcn_ds_read_tr16_b64_v4i16((LAS s16x4*)p));
  const u32x2 b = __builtin_bit_cast(u32x2, __builtin_amdgcn_ds_read_tr16_b64_v4i16((LAS s16x4*)(p + pitch4)));
  return u32x4{a[0], a[1], b[0], b[1]};
}
__device__ __forceinline__ f32x4 mfma16(u32x4 a, u32x4 b, f32x4 c) {
  return __builtin_amdgcn_mfma_f32_16x16x32_bf16(__builtin_bit_cast(bf16x8, a), __builtin_bit_cast(bf16x8, b), c, 0, 0, 0);
}
__global__ void __launch_bounds__(256) k_tail(const unsigned short* __restrict__ x, const unsigned short* __restrict__ wo,
                                              const unsigned short* __restrict__ wg, const unsigned short* __restrict__ wu,
                                              const unsigned short* __restrict__ wd, unsigned char* ws, unsigned short* __restrict__ out) {
  __shared__ __attribute__((aligned(16))) float hs[NB * DM];
  __shared__ __attribute__((aligned(16))) float red[4 * 256];
  __shared__ __attribute__((aligned(16))) float os[NB * DH];
  __shared__ __attribute__((aligned(16))) unsigned char wst[32768 + 64];
  __shared__ __attribute__((aligned(16))) unsigned short abuf[NB * FF + 8];
  const int tid = threadIdx.x, lane = tid & 63, wave = tid >> 6, bid = blockIdx.x;
  u64* g0 = (u64*)(ws + WS_G0);
  u64* g1 = (u64*)(ws + WS_G1);
  u64* g2 = (u64*)(ws + WS_G2);
  if (bid < 128) {
    const int j = bid;
    const int h = j >> 2, cq = j & 3;
    const int sb = tid >> 7, sd = tid & 127;
    const int p0 = (sb * NH + h) * NSPLIT;
    const f32x4 ml0 = *(const f32x4*)(ws + WS_PML + (size_t)p0 * 8), ml1 = *(const f32x4*)(ws + WS_PML + (size_t)p0 * 8 + 16);
    float po[4];
#pragma unroll
    for (int s = 0; s < 4; ++s) po[s] = ((const float*)(ws + WS_PO))[(size_t)(p0 + s) * DH + sd];
    const int cg = tid & 15, rg0 = tid >> 4;
    u32x4 wv[8];
#pragma unroll
    for (int i = 0; i < 8; ++i) wv[i] = __builtin_nontemporal_load((const u32x4*)(wo + (size_t)(h * DH + rg0 + 16 * i) * DM + cq * 128 + cg * 8));
    const unsigned short xr = x[((tid >> 3) & 1) * DM + (j & 63) * 8 + (tid & 7)];
    __builtin_amdgcn_sched_barrier(0);
    {
      const float M = fmaxf(fmaxf(ml0[0], ml0[2]), fmaxf(ml1[0], ml1[2]));
      const float w0 = __builtin_amdgcn_exp2f(ml0[0] - M), w1 = __builtin_amdgcn_exp2f(ml0[2] - M);
      const float w2 = __builtin_amdgcn_exp2f(ml1[0] - M), w3 = __builtin_amdgcn_exp2f(ml1[2] - M);
      const float L = fmaf(w3, ml1[3], fmaf(w2, ml1[1], fmaf(w1, ml0[3], w0 * ml0[1])));
      const float o = fmaf(w3, po[3], fmaf(w2, po[2], fmaf(w1, po[1], w0 * po[0])));
      os[tid] = rbf(o / L);
    }
    __syncthreads();
    {
      float a[16];
#pragma unroll
      for (int k = 0; k < 16; ++k) a[k] = 0.f;
#pragma unroll
      for (int i = 0; i < 8; ++i) {
        const float o0 = os[rg0 + 16 * i], o1 = os[DH + rg0 + 16 * i];
#pragma unroll
        for (int k = 0; k < 4; ++k) {
          a[2 * k] = fmaf(o0, lo16(wv[i][k]), a[2 * k]); a[2 * k + 1] = fmaf(o0, hi16(wv[i][k]), a[2 * k + 1]);
          a[8 + 2 * k] = fmaf(o1, lo16(wv[i][k]), a[8 + 2 * k]); a[8 + 2 * k + 1] = fmaf(o1, hi16(wv[i][k]), a[8 + 2 * k + 1]);
        }
      }
      TSTEP(8, lane & 32, x32)
      TSTEP(4, lane & 16, x16)
      *(f32x4*)(red + wave * 256 + (lane >> 5) * 128 + cg * 8 + ((lane >> 4) & 1) * 4) = f32x4{a[0], a[1], a[2], a[3]};
    }
    __syncthreads();
    {
      const float y = (red[tid] + red[256 + tid]) + (red[512 + tid] + red[768 + tid]);
      const int col = cq * 128 + sd;
      __hip_atomic_store(g0 + (((size_t)(col >> 3) * NH + h) * 2 + sb) * 8 + (col & 7), (1ull << 32) | (u64)__float_as_uint(y), RLX_AGENT);
    }
    if (j < 64) {
      u64 a0, a1; unsigned spins = 0;
      do {
        a0 = __hip_atomic_load(g0 + (size_t)j * 512 + tid, RLX_AGENT);
        a1 = __hip_atomic_load(g0 + (size_t)j * 512 + 256 + tid, RLX_AGENT);
      } while ((((a0 & a1) >> 32) != 1ull) && ++spins < SPIN_CAP);
      float s = __uint_as_float((unsigned)a0) + __uint_as_float((unsigned)a1);
      s = xrow_sum(s);
      if (lane < 16) os[wave * 16 + lane] = s;
      __syncthreads();
      if (tid < 16) {
        const float t = (os[tid] + os[16 + tid]) + (os[32 + tid] + os[48 + tid]);
        const float hv = rbf(bf2f(xr) + rbf(t));
        const float other = __builtin_bit_cast(float, __builtin_amdgcn_mov_dpp(__builtin_bit_cast(int, hv), XOR8, 0xf, 0xf, true));
        if (tid < 8) __hip_atomic_store(g1 + j * 8 + tid, (1ull << 32) | (__float_as_uint(other) & 0xffff0000u) | (__float_as_uint(hv) >> 16), RLX_AGENT);
      }
    } else {
      const int e = j - 64, jj = (e & 7) * 8 + (e >> 3);
      u32x4 d[7];
#pragma unroll
      for (int i = 0; i < 7; ++i) d[i] = __builtin_nontemporal_load((const u32x4*)(wd + (size_t)(wave * 448 + lane + 64 * i) * DM + jj * 8));
      if (tid < 8) abuf[NB * FF + tid] = 0;
      unsigned char* wl = wst + wave * 7168;
#pragma unroll
      for (int i = 0; i < 7; ++i) *(u32x4*)(wl + (lane + 64 * i) * 16) = d[i];
      asm volatile("s_waitcnt lgkmcnt(0)" ::: "memory");
      u32x4 bf[14];
      {
        const unsigned char* p = wl + (8 * (lane >> 4) + ((lane >> 2) & 3)) * 16 + (lane & 3) * 8;
#pragma unroll
        for (int s = 0; s < 14; ++s) bf[s] = tr_frag(p + s * 512, 64);
      }
      u64 av[7], hv; unsigned spins = 0; bool ok;
      do {
        ok = true;
#pragma unroll
        for (int i = 0; i < 7; ++i) { av[i] = __hip_atomic_load(g2 + tid + 256 * i, RLX_AGENT); }
        hv = __hip_atomic_load(g1 + jj * 8 + (tid & 7), RLX_AGENT);
#pragma unroll
        for (int i = 0; i < 7; ++i) ok &= (av[i] >> 32) == 1ull;
        ok &= (hv >> 32) == 1ull;
      } while (!ok && ++spins < SPIN_CAP);
#pragma unroll
      for (int i = 0; i < 7; ++i) { abuf[tid + 256 * i] = (unsigned short)av[i]; abuf[FF + tid + 256 * i] = (unsigned short)(av[i] >> 16); }
      __syncthreads();
      {
        const int m = lane & 15;
        const unsigned short* ap = m < 2 ? abuf + m * FF + wave * 448 + 8 * (lane >> 4) : abuf + NB * FF;
        const int st = m < 2 ? 32 : 0;
        f32x4 c0 = {0, 0, 0, 0}, c1 = {0, 0, 0, 0};
#pragma unroll
        for (int s = 0; s < 14; s += 2) {
          c0 = mfma16(*(const u32x4*)(ap + s * st), bf[s], c0);
          c1 = mfma16(*(const u32x4*)(ap + (s + 1) * st), bf[s + 1], c1);
        }
        c0 += c1;
        if (lane < 8) { red[wave * 16 + lane] = c0[0]; red[wave * 16 + 8 + lane] = c0[1]; }
      }
      __syncthreads();
      if (tid < 16) {
        const float s = (red[tid] + red[16 + tid]) + (red[32 + tid] + red[48 + tid]);
        const float hres = (tid >> 3) ? hi16((unsigned)hv) : lo16((unsigned)hv);
        out[(tid >> 3) * DM + jj * 8 + (tid & 7)] = f2bf(hres + rbf(s));
      }
    }
  } else {
    const int e = bid - 128, j = e < 96 ? ((e >> 5) * 8 + (e & 7)) * 4 + ((e >> 3) & 3) : (24 + ((e - 96) & 3)) * 4 + ((e - 96) >> 2);
    u32x4 g[4], u[4];
#pragma unroll
    for (int i = 0; i < 4; ++i) {
      const size_t off = (size_t)(wave * 128 + 32 * i + (lane >> 1)) * FF + j * 16 + (lane & 1) * 8;
      g[i] = __builtin_nontemporal_load((const u32x4*)(wg + off));
      u[i] = __builtin_nontemporal_load((const u32x4*)(wu + off));
    }
    if (tid < 8) abuf[NB * FF + tid] = 0;
    unsigned char* wl = wst + wave * 8192;
#pragma unroll
    for (int i = 0; i < 4; ++i) {
      *(u32x4*)(wl + (32 * i) * 32 + lane * 16) = g[i];
      *(u32x4*)(wl + 4096 + (32 * i) * 32 + lane * 16) = u[i];
    }
    asm volatile("s_waitcnt lgkmcnt(0)" ::: "memory");
    u32x4 bg[4], bu[4];
    {
      const unsigned char* p = wl + (8 * (lane >> 4) + ((lane >> 2) & 3)) * 32 + (lane & 3) * 8;
#pragma unroll
      for (int s = 0; s < 4; ++s) { bg[s] = tr_frag(p + s * 1024, 128); bu[s] = tr_frag(p + 4096 + s * 1024, 128); }
    }
    u64 a0, a1;
    {
      unsigned spins = 0;
      do {
        a0 = __hip_atomic_load(g1 + tid, RLX_AGENT);
        a1 = __hip_atomic_load(g1 + 256 + tid, RLX_AGENT);
      } while ((((a0 & a1) >> 32) != 1ull) && ++spins < SPIN_CAP);
      hs[tid] = lo16((unsigned)a0); hs[DM + tid] = hi16((unsigned)a0);
      hs[256 + tid] = lo16((unsigned)a1); hs[DM + 256 + tid] = hi16((unsigned)a1);
    }
    __syncthreads();
    {
      const f32x4 p0 = *(const f32x4*)(hs + lane * 8), p1 = *(const f32x4*)(hs + lane * 8 + 4);
      const f32x4 r0 = *(const f32x4*)(hs + DM + lane * 8), r1 = *(const f32x4*)(hs + DM + lane * 8 + 4);
      float s0 = ((p0[0] + p0[1]) + (p0[2] + p0[3])) + ((p1[0] + p1[1]) + (p1[2] + p1[3]));
      float s1 = ((r0[0] + r0[1]) + (r0[2] + r0[3])) + ((r1[0] + r1[1]) + (r1[2] + r1[3]));
      s0 = wave_sum(s0); s1 = wave_sum(s1);
      const float mean0 = s0 * (1.0f / 512.0f), mean1 = s1 * (1.0f / 512.0f);
      float q0 = 0.f, q1 = 0.f;
#pragma unroll
      for (int i = 0; i < 4; ++i) {
        float d0 = p0[i] - mean0, d1 = r0[i] - mean1; q0 += d0 * d0; q1 += d1 * d1;
        d0 = p1[i] - mean0; d1 = r1[i] - mean1; q0 += d0 * d0; q1 += d1 * d1;
      }
      q0 = wave_sum(q0); q1 = wave_sum(q1);
      const float mb0 = rbf(mean0), mb1 = rbf(mean1);
      const float den0 = rbf(sqrtf(rbf(rbf(q0 * (1.0f / 512.0f)) + 1e-5f)));
      const float den1 = rbf(sqrtf(rbf(rbf(q1 * (1.0f / 512.0f)) + 1e-5f)));
      abuf[tid] = f2bf(rbf(lo16((unsigned)a0) - mb0) / den0);
      abuf[256 + tid] = f2bf(rbf(lo16((unsigned)a1) - mb0) / den0);
      abuf[DM + tid] = f2bf(rbf(hi16((unsigned)a0) - mb1) / den1);
      abuf[DM + 256 + tid] = f2bf(rbf(hi16((unsigned)a1) - mb1) / den1);
    }
    __syncthreads();
    {
      const int m = lane & 15;
      const unsigned short* ap = m < 2 ? abuf + m * DM + wave * 128 + 8 * (lane >> 4) : abuf + NB * FF;
      const int st = m < 2 ? 32 : 0;
      f32x4 cg = {0, 0, 0, 0}, cu = {0, 0, 0, 0};
#pragma unroll
      for (int s = 0; s < 4; ++s) {
        const u32x4 af = *(const u32x4*)(ap + s * st);
        cg = mfma16(af, bg[s], cg); cu = mfma16(af, bu[s], cu);
      }
      if (lane < 16) *(f32x4*)(red + (wave * 16 + lane) * 4) = f32x4{cg[0], cu[0], cg[1], cu[1]};
    }
    __syncthreads();
    if (tid < 16) {
      const f32x4 t = (*(const f32x4*)(red + tid * 4) + *(const f32x4*)(red + (16 + tid) * 4)) + (*(const f32x4*)(red + (32 + tid) * 4) + *(const f32x4*)(red + (48 + tid) * 4));
      const float g0v = rbf(t[0]), u0 = rbf(t[1]), g1v = rbf(t[2]), u1 = rbf(t[3]);
      const unsigned short o0 = f2bf(rbf(g0v / (1.0f + __expf(-g0v))) * u0), o1 = f2bf(rbf(g1v / (1.0f + __expf(-g1v))) * u1);
      __hip_atomic_store(g2 + j * 16 + tid, (1ull << 32) | ((unsigned)o1 << 16) | (unsigned)o0, RLX_AGENT);
    }
  }
}

extern "C" void kernel_launch(void* const* d_in, const int* in_sizes, int n_in,
                              void* d_out, int out_size, void* d_ws, size_t ws_size,
                              hipStream_t stream) {
  const unsigned short* x = (const unsigned short*)d_in[0];
  const unsigned short* kc = (const unsigned short*)d_in[1];
  const unsigned short* vc = (const unsigned short*)d_in[2];
  const unsigned short* wq = (const unsigned short*)d_in[3];
  const unsigned short* wo = (const unsigned short*)d_in[4];
  const unsigned short* wg = (const unsigned short*)d_in[6];
  const unsigned short* wu = (const unsigned short*)d_in[7];
  const unsigned short* wd = (const unsigned short*)d_in[8];
  unsigned char* ws = (unsigned char*)d_ws;
  k_attn<<<256, NTHR, 0, stream>>>(x, kc, vc, wq, ws);
  k_tail<<<TAIL_BLOCKS, 256, 0, stream>>>(x, wo, wg, wu, wd, ws, (unsigned short*)d_out);
}
```
